```python
import torch

def _softmax(x, axis=-1):
    return torch.softmax(x, dim=axis)
def _gelu(x):
    return torch.nn.functional.gelu(x, approximate="tanh")
def _silu(x):
    return torch.nn.functional.silu(x)
def _sigmoid(x):
    return torch.sigmoid(x)
def _layernorm(x, axis=-1, eps=1e-5):
    m = x.mean(dim=axis, keepdim=True)
    v = x.var(dim=axis, keepdim=True, unbiased=False)
    return (x - m) / torch.sqrt(v + eps)
def _scatter_add(tgt, idx, src, axis=0):
    return tgt.clone().index_add_(axis, idx, src)
def _segment_sum(src, seg, n):
    out = torch.zeros((n,) + src.shape[1:], dtype=src.dtype)
    return out.index_add_(0, seg, src)
def _pad(x, pw):
    flat = tuple(v for pair in reversed(pw) for v in pair)
    return torch.nn.functional.pad(x, flat)

kernel_name = 'interleave_layers__grouped_kv_mqa__dense_activation_gelu__prompt'

def setup_inputs():

    _w = (lambda a: (lambda t: t.to(torch.bfloat16) if t.is_floating_point() else t)(torch.as_tensor(a)))
    def _A_setup():
        B, S, D, H = 1, 2048, 448, 16
        Dh = D // H
        return {
            "x": _w((lambda *s: torch.randn(*s, dtype=torch.bfloat16) * 0.05)(B, S, D)),
            "wq": _w((lambda *s: torch.randn(*s, dtype=torch.bfloat16) * 0.05)(D, H * Dh)),
            "wk": _w((lambda *s: torch.randn(*s, dtype=torch.bfloat16) * 0.05)(D, Dh)),
            "wv": _w((lambda *s: torch.randn(*s, dtype=torch.bfloat16) * 0.05)(D, Dh)),
            "wo": _w((lambda *s: torch.randn(*s, dtype=torch.bfloat16) * 0.05)(H * Dh, D)),
        }
    _A_in = _A_setup()

    def _B_setup():
        B, S, D, F = 1, 2048, 448, 1792
        return {
            "x": _w((lambda *s: torch.randn(*s, dtype=torch.bfloat16) * 0.05)(B, S, D)),
            "w1": _w((lambda *s: torch.randn(*s, dtype=torch.bfloat16) * 0.8)(D, F) * (D ** -0.5)),
            "b1": _w((lambda *s: torch.randn(*s, dtype=torch.bfloat16) * 12.8)(F) * 0.01),
            "w2": _w((lambda *s: torch.randn(*s, dtype=torch.bfloat16) * 0.2)(F, D) * (F ** -0.5)),
            "b2": _w((lambda *s: torch.randn(*s, dtype=torch.bfloat16) * 1.6)(D) * 0.01),
        }
    _B_in = _B_setup()

    return {
        "A_x": _A_in["x"],
        "A_wq": _A_in["wq"],
        "A_wk": _A_in["wk"],
        "A_wv": _A_in["wv"],
        "A_wo": _A_in["wo"],
        "B_x": _B_in["x"],
        "B_w1": _B_in["w1"],
        "B_b1": _B_in["b1"],
        "B_w2": _B_in["w2"],
        "B_b2": _B_in["b2"],
    }

def reference(A_x, A_wq, A_wk, A_wv, A_wo, B_x, B_w1, B_b1, B_w2, B_b2):
    def _A_ref(x, wq, wk, wv, wo):
        B, S, D = x.shape
        H = 16
        Dh = D // H
        q = (lambda x, *d: x.permute(*d))((x @ wq).reshape(B, S, H, Dh), 0, 2, 1, 3)
        k = x @ wk
        v = x @ wv
        scores = torch.einsum("bhqd,bkd->bhqk", q, k) * (Dh ** -0.5)
        p = _softmax(scores, axis=-1)
        o = torch.einsum("bhqk,bkd->bhqd", p, v)
        return (lambda x, *d: x.permute(*d))(o, 0, 2, 1, 3).reshape(B, S, H * Dh) @ wo

    def _B_ref(x, w1, b1, w2, b2):
        h = torch.einsum("bsd,df->bsf", x, w1) + b1
        h = _gelu(h)
        return torch.einsum("bsf,fd->bsd", h, w2) + b2

    x = A_x
    x = x + _A_ref(_layernorm(x, axis=-1), A_wq, A_wk, A_wv, A_wo)
    x = x + _B_ref(_layernorm(x, axis=-1), B_w1, B_b1, B_w2, B_b2)
    x = x + _A_ref(_layernorm(x, axis=-1), A_wq, A_wk, A_wv, A_wo)
    x = x + _B_ref(_layernorm(x, axis=-1), B_w1, B_b1, B_w2, B_b2)
    return x
```

```cpp
#include <hip/hip_runtime.h>
#include <hip/hip_bf16.h>
typedef unsigned short u16;
static __device__ __forceinline__ float b2f(u16 v){ return __uint_as_float(((unsigned)v)<<16);}
static __device__ __forceinline__ u16 f2b(float f){ unsigned u=__float_as_uint(f); u+=0x7fffu+((u>>16)&1u); return (u16)(u>>16);}
constexpr int S=2048,D=448,H=16,DH=28,F=1792;

__global__ void k_cvt(const u16* x, float* xf, int n){ int i=blockIdx.x*256+threadIdx.x; if(i<n) xf[i]=b2f(x[i]); }
__global__ void k_out(const float* xf, u16* o, int n){ int i=blockIdx.x*256+threadIdx.x; if(i<n) o[i]=f2b(xf[i]); }
__global__ void k_ln(const float* x, float* xn){
  int r=blockIdx.x; int t=threadIdx.x;
  float s=0; for(int i=t;i<D;i+=64) s+=x[r*D+i];
  for(int o=32;o;o>>=1) s+=__shfl_xor(s,o);
  float m=s/D; float v=0; for(int i=t;i<D;i+=64){ float d=x[r*D+i]-m; v+=d*d;}
  for(int o=32;o;o>>=1) v+=__shfl_xor(v,o);
  float rs=rsqrtf(v/D+1e-5f);
  for(int i=t;i<D;i+=64) xn[r*D+i]=b2f(f2b((x[r*D+i]-m)*rs));
}
__global__ void k_gemm(const float* A, const u16* W, const u16* bias, float* C, int K, int N, int mode){
  int n=blockIdx.x*64+threadIdx.x; int m=blockIdx.y; if(n>=N) return;
  float acc=0; for(int k=0;k<K;k++) acc+=A[m*K+k]*b2f(W[k*N+n]);
  if(bias) acc+=b2f(bias[n]);
  if(mode==1){ float u=0.7978845608f*(acc+0.044715f*acc*acc*acc); acc=0.5f*acc*(1.f+tanhf(u)); }
  if(mode==2) C[m*N+n]=b2f(f2b(C[m*N+n]+acc)); else C[m*N+n]=b2f(f2b(acc));
}
__global__ void k_attn(const float* q,const float* k,const float* v,float* o){
  int row=blockIdx.x*64+threadIdx.x;
  float qq[DH]; for(int d=0;d<DH;d++) qq[d]=q[row*DH+d]*0.18898223650461361f;
  float m=-1e30f,l=0,acc[DH]; for(int d=0;d<DH;d++) acc[d]=0;
  for(int j=0;j<S;j++){ float s=0; for(int d=0;d<DH;d++) s+=qq[d]*k[j*DH+d];
    float mn=fmaxf(m,s); float a=__expf(m-mn); float p=__expf(s-mn); l=l*a+p; for(int d=0;d<DH;d++) acc[d]=acc[d]*a+p*v[j*DH+d]; m=mn; }
  for(int d=0;d<DH;d++) o[row*DH+d]=b2f(f2b(acc[d]/l));
}
extern "C" void kernel_launch(void* const* d_in, const int* in_sizes, int n_in, void* d_out, int out_size, void* d_ws, size_t ws_size, hipStream_t stream){
  const u16* x=(const u16*)d_in[0]; const u16* wq=(const u16*)d_in[1]; const u16* wk=(const u16*)d_in[2]; const u16* wv=(const u16*)d_in[3]; const u16* wo=(const u16*)d_in[4];
  const u16* w1=(const u16*)d_in[6]; const u16* b1=(const u16*)d_in[7]; const u16* w2=(const u16*)d_in[8]; const u16* b2=(const u16*)d_in[9];
  float* ws=(float*)d_ws; float* xf=ws; float* xn=xf+S*D; float* q=xn+S*D; float* k=q+S*D; float* v=k+S*DH; float* o=v+S*DH; float* h=o+S*D;
  k_cvt<<<S*D/256,256,0,stream>>>(x,xf,S*D);
  for(int L=0;L<2;L++){
    k_ln<<<S,64,0,stream>>>(xf,xn);
    k_gemm<<<dim3(7,S),64,0,stream>>>(xn,wq,nullptr,q,D,D,0);
    k_gemm<<<dim3(1,S),64,0,stream>>>(xn,wk,nullptr,k,D,DH,0);
    k_gemm<<<dim3(1,S),64,0,stream>>>(xn,wv,nullptr,v,D,DH,0);
    k_attn<<<S*H/64,64,0,stream>>>(q,k,v,o);
    k_gemm<<<dim3(7,S),64,0,stream>>>(o,wo,nullptr,xf,D,D,2);
    k_ln<<<S,64,0,stream>>>(xf,xn);
    k_gemm<<<dim3(28,S),64,0,stream>>>(xn,w1,b1,h,D,F,1);
    k_gemm<<<dim3(7,S),64,0,stream>>>(h,w2,b2,xf,F,D,2);
  }
  k_out<<<S*D/256,256,0,stream>>>(xf,(u16*)d_out,S*D);
}
```

```cpp
#include <hip/hip_runtime.h>
#include <hip/hip_bf16.h>

typedef unsigned short u16;
typedef unsigned u32;
typedef unsigned long long u64;
typedef u32 u32x2 __attribute__((ext_vector_type(2)));
typedef u32 u32x4 __attribute__((ext_vector_type(4)));
typedef u32 u32x16 __attribute__((ext_vector_type(16)));
typedef u32 u32x32 __attribute__((ext_vector_type(32)));
typedef float f32x2 __attribute__((ext_vector_type(2)));
typedef float f32x4 __attribute__((ext_vector_type(4)));
typedef float f32x16 __attribute__((ext_vector_type(16)));
typedef __bf16 bf16x2 __attribute__((ext_vector_type(2)));
typedef __bf16 bf16x8 __attribute__((ext_vector_type(8)));
typedef short s16x4 __attribute__((ext_vector_type(4)));
#define DI __device__ __forceinline__
#define GAS __attribute__((address_space(1)))
#define LAS __attribute__((address_space(3)))
#define RLX_AGENT __ATOMIC_RELAXED, __HIP_MEMORY_SCOPE_AGENT
typedef __amdgpu_buffer_rsrc_t rsrc_t;

constexpr int D_ = 448, F_ = 1792, DH = 28;
constexpr int SC1 = 16;

constexpr int EPOCH_OFF = 0;
constexpr int DEAD_OFF = 128;
constexpr int KVF_OFF = 1024;
constexpr int FLAG_OFF = KVF_OFF + 2 * 64 * 128;
constexpr int W1F_OFF = FLAG_OFF + 256 * 8 * 128;
constexpr int W2F_OFF = W1F_OFF + 56 * 28 * 1024;
constexpr int WOF_OFF = W2F_OFF + 56 * 28 * 1024;
constexpr int WQF_OFF = WOF_OFF + 28 * 16 * 1024;
constexpr int WKVF_OFF = WQF_OFF + 16 * 28 * 1024;
constexpr int CS_OFF = WKVF_OFF + 56 * 1024;
constexpr int KV_OFF = CS_OFF + 1792 * 64;
constexpr int XG = 56 * 512;
constexpr int XNF_OFF = KV_OFF + 2 * 262144;
constexpr int XF_OFF = XNF_OFF + 64 * XG;
constexpr int X1F_OFF = XF_OFF + 64 * XG;
constexpr int SX_OFF = X1F_OFF + 2 * 64 * XG;
constexpr int OF_OFF = SX_OFF + 2 * 64 * 7168;
constexpr int PF_OFF = OF_OFF + 2 * 64 * 32768;
constexpr int PG = 16 * 7168;
constexpr int DBG_OFF = PF_OFF + 2 * 64 * PG;
constexpr int WS_TOTAL = DBG_OFF + 256 * 8 * 64 * 8;
enum { E_X = 0, E_O = 1, E_X1 = 2, E_P = 3 };

constexpr int L_RING = 0, L_EXT = 98304, L_XN = 100352, L_EX = 129024, L_ST = 161792, L_BYTES = 163840;
constexpr int L_OF = 0, L_H = 0, L_STASH = 32768, L_KVX = 65536;
constexpr int L_BAD = L_ST + 512, L_RDY = L_ST + 544, L_SUM = L_ST + 576, L_RSTD = L_ST + 832, L_STAMP = L_ST + 1024;
constexpr int P_WQ = 0, P_WKV = 100352, P_TILE = 125952, P_PART = 142336, P_STAT = 158720;

constexpr float QSCALE = 0.18898223650461361f * 1.4426950408889634f;

DI float bf_lo(u32 v) { return __uint_as_float(v << 16); }
DI float bf_hi(u32 v) { return __uint_as_float(v & 0xffff0000u); }
DI u32 pk(float a, float b) { f32x2 t = {a, b}; return __builtin_bit_cast(u32, __builtin_convertvector(t, bf16x2)); }
DI f32x16 mfma32(u32x4 a, u32x4 b, f32x16 c) { return __builtin_amdgcn_mfma_f32_32x32x16_bf16(__builtin_bit_cast(bf16x8, a), __builtin_bit_cast(bf16x8, b), c, 0, 0, 0); }
DI f32x4 mfma16(u32x4 a, u32x4 b, f32x4 c) { return __builtin_amdgcn_mfma_f32_16x16x32_bf16(__builtin_bit_cast(bf16x8, a), __builtin_bit_cast(bf16x8, b), c, 0, 0, 0); }
DI u32x4 ldf(rsrc_t rs, int l16, int base, int i) { return __builtin_amdgcn_raw_buffer_load_b128(rs, l16 + (i & 3) * 1024, base + (i >> 2) * 4096, SC1); }
DI u32x4 ldg_sc1(rsrc_t rs, int voff, int soff) { return __builtin_amdgcn_raw_buffer_load_b128(rs, voff, soff, SC1); }
DI u32x2 ldg64_sc1(rsrc_t rs, int voff, int soff) { return __builtin_amdgcn_raw_buffer_load_b64(rs, voff, soff, SC1); }
DI void stg_sc1(u32x4 v, rsrc_t rs, int voff, int soff) { __builtin_amdgcn_raw_buffer_store_b128(v, rs, voff + soff, 0, SC1); }
DI void stg64_sc1(u32x2 v, rsrc_t rs, int voff, int soff) { __builtin_amdgcn_raw_buffer_store_b64(v, rs, voff, soff, SC1); }
DI void stg32_sc1(u32 v, rsrc_t rs, int voff, int soff) { __builtin_amdgcn_raw_buffer_store_b32(v, rs, voff, soff, SC1); }
DI u32x4 lds128(LAS unsigned char* L, int off) { return *(LAS u32x4*)(L + off); }
DI void sts128(LAS unsigned char* L, int off, u32x4 v) { *(LAS u32x4*)(L + off) = v; }
DI u32x4 tr_frag(LAS unsigned char* p, int lo, int hi) {
  const u32x2 a = __builtin_bit_cast(u32x2, __builtin_amdgcn_ds_read_tr16_b64_v4i16((LAS s16x4*)(p + lo)));
  const u32x2 c = __builtin_bit_cast(u32x2, __builtin_amdgcn_ds_read_tr16_b64_v4i16((LAS s16x4*)(p + hi)));
  return u32x4{a[0], a[1], c[0], c[1]};
}
#define SBAR() __builtin_amdgcn_sched_barrier(0)
#define PIN(x) asm volatile("" : "+v"(x))
#define BAR() asm volatile("s_waitcnt lgkmcnt(0)\n\ts_barrier" ::: "memory")
#define WAIT_BAR(N) asm volatile("s_waitcnt vmcnt(%0) lgkmcnt(0)\n\ts_barrier" :: "n"(N) : "memory")
#define FENCE() asm volatile("" ::: "memory")
#define YIELD() __builtin_amdgcn_s_sleep(2)
template <int N>
DI void dma(u32x4 rs, u32 voff, u32 soff, u32 lds) {
  static_assert(N >= 1 && N <= 4, "the instruction offset reaches 4095");
  u32 keep;
#define DMA_OPS : "=&s"(keep) : "v"(voff), "s"(rs), "s"(soff), "s"(lds) : "memory"
#define DMA_HEAD "s_mov_b32 %0, m0\n\ts_mov_b32 m0, %4\n\ts_nop 0\n\t"
#define DMA_P(o) "buffer_load_dwordx4 %1, %2, %3 offen offset:" #o " sc1 lds\n\t"
  if constexpr (N == 1) asm volatile(DMA_HEAD DMA_P(0) "s_mov_b32 m0, %0" DMA_OPS);
  if constexpr (N == 2) asm volatile(DMA_HEAD DMA_P(0) DMA_P(1024) "s_mov_b32 m0, %0" DMA_OPS);
  if constexpr (N == 3) asm volatile(DMA_HEAD DMA_P(0) DMA_P(1024) DMA_P(2048) "s_mov_b32 m0, %0" DMA_OPS);
  if constexpr (N == 4) asm volatile(DMA_HEAD DMA_P(0) DMA_P(1024) DMA_P(2048) DMA_P(3072) "s_mov_b32 m0, %0" DMA_OPS);
}
DI void touch(u32x4 rs, u32 voff, u32 soff, u32 lds) {
  u32 keep;
  asm volatile("s_mov_b32 %0, m0\n\ts_mov_b32 m0, %4\n\ts_nop 0\n\tbuffer_load_dword %1, %2, %3 offen lds\n\ts_mov_b32 m0, %0" DMA_OPS);
}
DI u32x4 make_srd(const void* p, u32 bytes) { const u64 a = (u64)p; return u32x4{(u32)a, (u32)(a >> 32) & 0xffffu, bytes, 0x00020000u}; }
DI float swap32_sum(float a, float b) { auto r = __builtin_amdgcn_permlane32_swap(__float_as_uint(a), __float_as_uint(b), false, false); return __uint_as_float(r[0]) + __uint_as_float(r[1]); }
DI float swap16_sum(float a, float b) { auto r = __builtin_amdgcn_permlane16_swap(__float_as_uint(a), __float_as_uint(b), false, false); return __uint_as_float(r[0]) + __uint_as_float(r[1]); }
template <int CTRL> DI float dpp_sum(float v) { return v + __uint_as_float(__builtin_amdgcn_update_dpp(0, __float_as_uint(v), CTRL, 0xf, 0xf, false)); }
DI float sum8(float v) { return dpp_sum<0x141>(dpp_sum<0x4e>(dpp_sum<0xb1>(v))); }
DI float xor32_max(float v) { auto r = __builtin_amdgcn_permlane32_swap(__float_as_uint(v), __float_as_uint(v), false, false); return fmaxf(__uint_as_float(r[0]), __uint_as_float(r[1])); }

constexpr u32 SPIN_CAP = 1u << 17;
DI bool spin_over(unsigned char* ws, u32& spins, u64 tag) {
  ++spins;
  if ((spins & 1023u) != 0u) return false;
  GAS u64* dead = (GAS u64*)(ws + DEAD_OFF);
  if (__hip_atomic_load(dead, RLX_AGENT) == tag) return true;
  if (spins > SPIN_CAP) { __hip_atomic_store(dead, tag, RLX_AGENT); return true; }
  return false;
}
DI void signal_and_wait(unsigned char* ws, int edge, int g, int j, int lane, u64 tag) {
  GAS u32* f = (GAS u32*)(ws + FLAG_OFF + (g * 32 + edge) * 128);
  if (lane < 4) __hip_atomic_store(f + lane * 256 + j, 1u, RLX_AGENT);
  for (u32 spins = 0;;) {
    const u32 v = __hip_atomic_load(f + j * 256 + (lane & 3), RLX_AGENT);
    if (__all(v == 1u) || spin_over(ws, spins, tag)) break;
  }
  __builtin_amdgcn_fence(__ATOMIC_ACQUIRE, "wavefront");
}

#define STAMP(i)
#define STAMP_DUMP()

struct In { const u16 *x, *wq, *wk, *wv, *wo, *w1, *b1, *w2, *b2; };

template <bool G>
DI void first_phase(const In& in, unsigned char* ws, rsrc_t rs, LAS unsigned char* L, int g, int j, int w, int lane, u32x4 (&wqf)[14], u64 tag) {
  const u32 lds0 = (u32)(size_t)L;
  const int gi = lane >> 4, q = (lane & 15) >> 2, p = lane & 3;
  u32x4 xv[4];
  if constexpr (G) {
#pragma unroll
    for (int i = 0; i < 4; ++i) xv[i] = *(const u32x4*)(in.x + (size_t)(g * 32 + 8 * i + (lane >> 3)) * D_ + (8 * w + (lane & 7)) * 8);
  }
  const int id = g * 4 + j;
  const void* src; u32 sbytes; int soff, pitch, lpr, pl, base, c1, c2, c3, dhi, df, dst, dstep, zm = 0, zt = 64; bool swp = false;
  int type;
  {
    const int r = id < 6 ? id - 2 : id - 4;
    type = (id & ~5) == 0 ? 4 : r < 98 ? 0 : r < 196 ? 1 : r < 224 ? 2 : 3;
    if (type == 0) {
      const int kt = r / 14, nt = r % 14;
      src = in.w1; sbytes = D_ * F_ * 2; soff = (64 * kt * F_ + 128 * nt) * 2; pitch = F_ * 2; lpr = 16; pl = 256;
      base = P_TILE + 32 * (w >> 2) * 256 + 64 * (w & 3); c1 = 8; c2 = 0; c3 = 32; dhi = 4 * 256; df = 16 * 256;
      dst = W1F_OFF + ((4 * nt + (w & 3)) * 28 + 4 * kt + 2 * (w >> 2)) * 1024; dstep = 1024;
    } else if (type == 1) {
      const int e = r - 98, rt = e / 7, ct = e % 7, fs = 8 * rt + w;
      src = in.w2; sbytes = D_ * F_ * 2; soff = (128 * rt * D_ + 64 * ct) * 2; pitch = D_ * 2; lpr = 8; pl = 128;
      base = P_TILE + 16 * w * 128; c1 = 4; c2 = 0; c3 = 32; dhi = 8 * 128; df = 64; swp = true;
      dst = W2F_OFF + ((fs / 28 * 14 + 2 * ct) * 28 + fs % 28) * 1024; dstep = 28 * 1024;
    } else if (type == 2) {
      const int e = r - 196, hq = e / 7, ct = e % 7;
      src = in.wo; sbytes = D_ * D_ * 2; soff = (112 * hq * D_ + 64 * ct) * 2; pitch = D_ * 2; lpr = 8; pl = 128;
      base = P_TILE + 28 * (w >> 1) * 128 + 64 * (w & 1); c1 = 16; c2 = 4; c3 = 0; dhi = 8 * 128; df = 32;
      dst = WOF_OFF + ((4 * ct + 2 * (w & 1)) * 16 + 4 * hq + (w >> 1)) * 1024; dstep = 16 * 1024;
    } else if (type == 3) {
      const int e = r - 224, kt = e >> 2, hq = e & 3;
      src = in.wq; sbytes = D_ * D_ * 2; soff = (64 * kt * D_ + 112 * hq) * 2; pitch = D_ * 2; lpr = 14; pl = 224;
      base = P_TILE + 32 * (w >> 2) * 224 + 56 * (w & 3); c1 = 8; c2 = 0; c3 = 32; dhi = 4 * 224; df = 16 * 224; zm = 31; zt = DH;
      dst = WQF_OFF + ((4 * hq + (w & 3)) * 28 + 4 * kt + 2 * (w >> 2)) * 1024; dstep = 1024;
    } else {
      const int t = id >> 2;
      src = in.wq; sbytes = 0; soff = 0; pitch = 0; lpr = 16; pl = 56;
      base = P_WKV + 64 * (w < 7 ? w : 0) * 56 + 32 * t; c1 = 16; c2 = 8; c3 = 0; dhi = 4 * 56; df = 32 * 56; zm = 15; zt = DH - 16 * t;
      dst = w < 7 ? WKVF_OFF + ((2 * j + t) * 14 + 2 * w) * 1024 : WS_TOTAL; dstep = 1024;
    }
  }
  {
    const u32x4 st = make_srd(src, sbytes);
    const u32 mul = lpr == 14 ? 74899u : lpr == 16 ? 65536u : 131072u;
#pragma unroll
    for (int i = 0; i < 2; ++i) {
      const u32 idx = (2 * w + i) * 64 + lane, row = __umul24(idx, mul) >> 20;
      dma<1>(st, row * pitch + (idx - row * lpr) * 16, soff, lds0 + P_TILE + (2 * w + i) * 1024);
    }
    const u32x4 sk = make_srd(j ? in.wv : in.wk, j < 2 ? D_ * DH * 2 : 0);
    dma<3>(sk, lane * 16, w * 3072, lds0 + P_WKV + w * 3072);
    dma<1>(sk, lane * 16, 24576, lds0 + P_WKV + 24576);
  }
  const u32x4 sq = make_srd(in.wq, D_ * D_ * 2);
  if constexpr (!G) {
#pragma unroll
    for (int i = 0; i < 4; ++i) touch(sq, lane * 64, (blockIdx.x >> 3) * 12544 + i * 4096, lds0 + P_PART);
  }
#define WQ_DMA(a, z) _Pragma("unroll") for (int i = (a); i < (z); ++i) { const int pc = i < 12 ? i * 8 + w : 96 + (w & 1); \
    const u32 idx = pc * 64 + lane, row = __umul24(idx, 74899u) >> 20; dma<1>(sq, row * (D_ * 2) + (idx - row * 14) * 16, j * 224, lds0 + P_WQ + pc * 1024); }
  f32x2 v[4][4];
  if constexpr (G) {
#pragma unroll
    for (int i = 0; i < 4; ++i) {
      f32x2 s1 = {0.f, 0.f}, s2 = {0.f, 0.f};
#pragma unroll
      for (int e = 0; e < 4; ++e) { v[i][e] = f32x2{bf_lo(xv[i][e]), bf_hi(xv[i][e])}; s1 += v[i][e]; s2 += v[i][e] * v[i][e]; }
      *(LAS f32x2*)(L + P_STAT + ((8 * i + (lane >> 3)) * 8 + w) * 8) = f32x2{sum8(s1[0] + s1[1]), sum8(s2[0] + s2[1])};
    }
  } else {
    *(LAS f32x2*)(L + P_STAT + ((lane & 31) * 8 + 7) * 8) = f32x2{0.f, 0.f};
  }
  WAIT_BAR(0);
  STAMP(27);
  WQ_DMA(0, 4);
  u32x4 f0, f1, wkv[7];
  {
    LAS unsigned char* a = L + base + ((gi >> 1) * c1 + (gi & 1) * c2 + q) * pl + (gi & 1) * c3 + 8 * (swp ? ((p >> 1) | ((p & 1) << 1)) : p);
    f0 = tr_frag(a, 0, dhi); f1 = tr_frag(a + df, 0, dhi);
    LAS unsigned char* k = L + P_WKV + (112 * (w & 3) + 8 * (gi >> 1) + q) * 56 + 32 * (gi & 1) + 8 * p;
    asm volatile("" : "+v"(k));
#pragma unroll
    for (int i = 0; i < 7; ++i) wkv[i] = tr_frag(k, i * 896, i * 896 + 224);
  }
  u32x4 xn[4];
  if constexpr (G) {
#pragma unroll
    for (int i = 0; i < 4; ++i) {
      const f32x2 t = *(LAS f32x2*)(L + P_STAT + (8 * i * 8 + lane) * 8);
      const float mean = sum8(t[0]) * (1.f / D_);
      const float rstd = __builtin_amdgcn_rsqf(fmaxf(sum8(t[1]) * (1.f / D_) - mean * mean, 0.f) + 1e-5f), nb = -mean * rstd;
#pragma unroll
      for (int e = 0; e < 4; ++e) { const f32x2 y = v[i][e] * rstd + nb; xn[i][e] = pk(y[0], y[1]); }
    }
  }
  BAR();
  if constexpr (G) {
#pragma unroll
    for (int i = 0; i < 4; ++i) sts128(L, L_XN + (8 * w + (lane & 7)) * 512 + ((8 * i + (lane >> 3)) ^ (lane & 7)) * 16, xn[i]);
  }
  if ((lane & zm) >= zt) { f0 = u32x4{0u, 0u, 0u, 0u}; f1 = f0; }
  if (type == 2 && gi == 3) { f0[2] = f0[3] = 0u; f1[2] = f1[3] = 0u; }
  stg_sc1(f0, rs, lane * 16, dst);
  stg_sc1(f1, rs, lane * 16, dst + dstep);
  BAR();
  const f32x16 zero = {};
  if (type == 0) {
    const u32x4 ones = {0x3f803f80u, 0x3f803f80u, 0x3f803f80u, 0x3f803f80u};
    const f32x16 c = mfma32(ones, f1, mfma32(ones, f0, zero));
    const int e = (dst - W1F_OFF) >> 10;
    if (lane < 32) stg32_sc1(__float_as_uint(c[0]), rs, lane * 64, CS_OFF + (e / 28) * 2048 + ((e % 28) >> 1) * 4);
  }
  if (j < 2 && w < 4) {
    const int r = lane & 31, h = lane >> 5, tk = j ? ((r & 19) | ((r & 4) << 1) | ((r & 8) >> 1)) : r;
    u32x4 xf[7];
#pragma unroll
    for (int i = 0; i < 7; ++i) { const int c = 14 * w + 2 * i + h; xf[i] = lds128(L, L_XN + c * 512 + (tk ^ (c & 7)) * 16); }
    f32x16 acc = zero;
    if (j == 0) {
#pragma unroll
      for (int i = 0; i < 7; ++i) acc = mfma32(wkv[i], xf[i], acc);
    } else {
#pragma unroll
      for (int i = 0; i < 7; ++i) acc = mfma32(xf[i], wkv[i], acc);
    }
#pragma unroll
    for (int i = 0; i < 4; ++i) *(LAS f32x4*)(L + P_PART + w * 4096 + i * 1024 + lane * 16) = f32x4{acc[4 * i], acc[4 * i + 1], acc[4 * i + 2], acc[4 * i + 3]};
  }
  BAR();
  if (j < 2 && w < 2) {
    f32x4 a = {0.f, 0.f, 0.f, 0.f}, c = a;
#pragma unroll
    for (int i = 0; i < 4; ++i) { a += *(LAS f32x4*)(L + P_PART + i * 4096 + w * 2048 + lane * 16); c += *(LAS f32x4*)(L + P_PART + i * 4096 + w * 2048 + 1024 + lane * 16); }
    u32x4 o = {pk(a[0], a[1]), pk(a[2], a[3]), pk(c[0], c[1]), pk(c[2], c[3])};
    if (j == 0) { if (w == 1 && lane >= 32) { o[2] = 0x3f803f80u; o[3] = 0x00003f80u; } }
    else if ((lane & 31) >= DH) { const u32 u = (lane & 31) == DH ? 0x3f803f80u : 0u; o = u32x4{u, u, u, u}; }
    stg_sc1(o, rs, lane * 16, KV_OFF + (((g + 62 + (j ? 1 + w : 0)) & 63) * 4 + 2 * j + w) * 1024);
  }
  STAMP(28);
  WQ_DMA(4, 9);
  WAIT_BAR(5);
  STAMP(29);
  if constexpr (!G) { if (lane == 0) __hip_atomic_store((GAS u64*)(ws + KVF_OFF) + g * 16 + j, tag, RLX_AGENT); } else YIELD();
  WQ_DMA(9, 13);
  WAIT_BAR(0);
  LAS unsigned char* a = L + P_WQ + (224 * (w >> 2) + 8 * (gi >> 1) + q) * 224 + 56 * (w & 3) + 32 * (gi & 1) + 8 * p;
  asm volatile("" : "+v"(a));
#pragma unroll
  for (int i = 0; i < 14; ++i) wqf[i] = tr_frag(a, i * 3584, i * 3584 + 896);
}

template <bool G>
DI void ln_kv_tail(u32x4 xb, const u32x4 (&wkv)[7], rsrc_t rs, LAS unsigned char* L, int g, int j, int w, int lane) {
  const int tok = lane & 7, c = 8 * w + (lane >> 3);
  float v[8];
  if constexpr (G) {
    stg_sc1(xb, rs, (g * 32 + 8 * j + tok) * (D_ * 2) + c * 16, XF_OFF);
#pragma unroll
    for (int i = 0; i < 4; ++i) { v[2 * i] = bf_lo(xb[i]); v[2 * i + 1] = bf_hi(xb[i]); }
    float s1 = ((v[0] + v[1]) + (v[2] + v[3])) + ((v[4] + v[5]) + (v[6] + v[7]));
    float s2 = ((v[0] * v[0] + v[1] * v[1]) + (v[2] * v[2] + v[3] * v[3])) + ((v[4] * v[4] + v[5] * v[5]) + (v[6] * v[6] + v[7] * v[7]));
    float s = swap32_sum(s1, s2);
    s = dpp_sum<0x128>(swap16_sum(s, s));
    *(LAS float*)(L + L_ST + (tok * 8 + w) * 8 + (lane >> 5) * 4) = s;
  }
  BAR();
  if constexpr (G) {
    const f32x4 p0 = *(LAS f32x4*)(L + L_ST + tok * 64), p1 = *(LAS f32x4*)(L + L_ST + tok * 64 + 16), p2 = *(LAS f32x4*)(L + L_ST + tok * 64 + 32);
    const f32x2 p3 = *(LAS f32x2*)(L + L_ST + tok * 64 + 48);
    const float s1 = ((p0[0] + p0[2]) + (p1[0] + p1[2])) + ((p2[0] + p2[2]) + p3[0]), s2 = ((p0[1] + p0[3]) + (p1[1] + p1[3])) + ((p2[1] + p2[3]) + p3[1]);
    const float mean = s1 * (1.f / D_);
    const float var = fmaxf(s2 * (1.f / D_) - mean * mean, 0.f);
    const float rstd = __builtin_amdgcn_rsqf(var + 1e-5f);
    u32x4 xn;
#pragma unroll
    for (int i = 0; i < 4; ++i) xn[i] = pk((v[2 * i] - mean) * rstd, (v[2 * i + 1] - mean) * rstd);
    sts128(L, L_KVX + (c * 8 + tok) * 16, xn);
    stg_sc1(xn, rs, c * 512 + ((8 * j + tok) ^ (lane >> 3)) * 16, XNF_OFF + g * XG);
  }
  BAR();
  f32x4 acc = {0.f, 0.f, 0.f, 0.f};
  {
    u32x4 xf[7];
    LAS unsigned char* p = L + L_KVX + (((w >> 2) * 28 + (lane >> 4)) * 8 + (lane & 7)) * 16;
    asm volatile("" : "+v"(p));
#pragma unroll
    for (int i = 0; i < 7; ++i) xf[i] = lds128(p, i * 512);
    if ((w & 2) == 0) {
#pragma unroll
      for (int i = 0; i < 7; ++i) acc = mfma16(wkv[i], xf[i], acc);
    } else {
#pragma unroll
      for (int i = 0; i < 7; ++i) acc = mfma16(xf[i], wkv[i], acc);
    }
    if (w >= 4) *(LAS f32x4*)(L + L_EX + ((w & 3) * 64 + lane) * 16) = acc;
  }
  BAR();
  if (G && w < 4) {
    acc += *(LAS f32x4*)(L + L_EX + (w * 64 + lane) * 16);
    const int c16 = lane & 15, rg = lane >> 4;
    u32x2 r = {pk(acc[0], acc[1]), pk(acc[2], acc[3])};
    int off; bool on;
    if (w < 2) {
      if (w == 1 && rg == 3) r = u32x2{0x3f803f80u, 0x00003f80u};
      off = (((g + 62) & 63) * 4 + w) * 1024 + ((rg & 1) * 32 + 8 * j + c16) * 16 + (rg >> 1) * 8; on = c16 < 8;
    } else {
      if (w == 3 && c16 >= 12) r = c16 == 12 ? u32x2{0x3f803f80u, 0x3f803f80u} : u32x2{0u, 0u};
      const int s = j >> 1, unit = s ? g : ((g + 63) & 63);
      off = (unit * 4 + 2 + s) * 1024 + ((j & 1) * 32 + (w - 2) * 16 + c16) * 16 + rg * 8; on = rg < 2;
    }
    if (on) stg64_sc1(r, rs, off, KV_OFF + 262144);
  }
}

DI u32 gelu_pk(float x0, float x1, float rstd) {
  const f32x2 x = f32x2{x0, x1} * rstd;
  const f32x2 z = x * (x * x * -0.10294324f + -2.3022082f);
  const f32x2 d = f32x2{__builtin_amdgcn_exp2f(z[0]), __builtin_amdgcn_exp2f(z[1])} + 1.f;
  const f32x2 y = x * f32x2{__builtin_amdgcn_rcpf(d[0]), __builtin_amdgcn_rcpf(d[1])};
  return pk(y[0], y[1]);
}
#define ATT_ASM_G \
  "s_mov_b32 %[keep], m0\nv_mov_b32 v240, 0\nv_mov_b32 v241, 0\nv_mov_b32 v242, 0\n" \
  "v_mov_b32 v243, 0\nv_mov_b32 v244, 0\nv_mov_b32 v245, 0\nv_mov_b32 v246, 0\n" \
  "v_mov_b32 v247, 0\nv_mov_b32 v248, 0\nv_mov_b32 v249, 0\nv_mov_b32 v250, 0\n" \
  "v_mov_b32 v251, 0\nv_mov_b32 v252, 0\nv_mov_b32 v253, 0\nv_mov_b32 v254, 0\n" \
  "v_mov_b32 v255, 0\ns_waitcnt vmcnt(0) lgkmcnt(0)\ns_barrier\nds_read_b128 v[176:179], %[lr2]\n" \
  "ds_read_b128 v[180:183], %[lr2] offset:1024\nds_read_b128 v[192:195], %[lr2] offset:4096\nds_read_b128 v[196:199], %[lr2] offset:5120\nds_read_b128 v[200:203], %[lr2] offset:6144\n" \
  "s_waitcnt lgkmcnt(3)\nv_mfma_f32_32x32x16_bf16 v[224:239], v[176:179], v[172:175], 0\nv_mfma_f32_32x32x16_bf16 v[224:239], v[180:183], v[168:171], v[224:239]\ns_waitcnt lgkmcnt(0)\n" \
  "s_barrier\ns_nop 9\ns_setprio 3\nv_exp_f32 v224, v224\n" \
  "v_exp_f32 v225, v225\nds_read_b128 v[188:191], %[lr0] offset:3072\nds_read_b128 v[176:179], %[lr0] offset:0\nv_exp_f32 v226, v226\n" \
  "v_exp_f32 v227, v227\nv_exp_f32 v228, v228\nv_mfma_f32_32x32x16_bf16 v[208:223], v[192:195], v[172:175], 0\nds_read_b128 v[180:183], %[lr0] offset:1024\n" \
  "ds_read_b128 v[184:187], %[lr0] offset:2048\nv_exp_f32 v229, v229\nv_exp_f32 v230, v230\nv_exp_f32 v231, v231\n" \
  "v_mfma_f32_32x32x16_bf16 v[208:223], v[196:199], v[168:171], v[208:223]\ns_add_u32 %[ko], %[ko], 0x4000\ns_and_b32 %[ko], %[ko], 0x3ffff\ns_add_u32 m0, %[m], 32768\n" \
  "s_nop 0\nbuffer_load_dwordx4 %[l16], %[rkv], %[ko] offen offset:0 sc1 lds\nbuffer_load_dwordx4 %[l16], %[rkv], %[ko] offen offset:1024 sc1 lds\nv_exp_f32 v232, v232\n" \
  "v_exp_f32 v233, v233\nv_exp_f32 v234, v234\nv_exp_f32 v235, v235\nv_smfmac_f32_32x32x32_bf16 v[240:255], v[200:203], v[224:231], %[idx]\n" \
  "v_exp_f32 v236, v236\nv_exp_f32 v237, v237\nv_exp_f32 v238, v238\nv_exp_f32 v239, v239\n" \
  "s_setprio 2\nv_exp_f32 v208, v208\nv_exp_f32 v209, v209\ns_waitcnt lgkmcnt(0)\n" \
  "v_smfmac_f32_32x32x32_bf16 v[240:255], v[188:191], v[232:239], %[idx]\nds_read_b128 v[204:207], %[lr0] offset:7168\nds_read_b128 v[192:195], %[lr0] offset:4096\nv_exp_f32 v210, v210\n" \
  "v_exp_f32 v211, v211\nv_exp_f32 v212, v212\nv_mfma_f32_32x32x16_bf16 v[224:239], v[176:179], v[172:175], 0\nds_read_b128 v[196:199], %[lr0] offset:5120\n" \
  "ds_read_b128 v[200:203], %[lr0] offset:6144\nv_exp_f32 v213, v213\nv_exp_f32 v214, v214\nv_exp_f32 v215, v215\n" \
  "v_mfma_f32_32x32x16_bf16 v[224:239], v[180:183], v[168:171], v[224:239]\nbuffer_load_dwordx4 %[l16], %[rkv], %[ko] offen offset:2048 sc1 lds\nbuffer_load_dwordx4 %[l16], %[rkv], %[ko] offen offset:3072 sc1 lds\nv_exp_f32 v216, v216\n" \
  "v_exp_f32 v217, v217\nv_exp_f32 v218, v218\nv_exp_f32 v219, v219\nv_smfmac_f32_32x32x32_bf16 v[240:255], v[184:187], v[208:215], %[idx]\n" \
  "v_exp_f32 v220, v220\nv_exp_f32 v221, v221\nv_exp_f32 v222, v222\nv_exp_f32 v223, v223\n" \
  "s_setprio 1\nv_exp_f32 v224, v224\nv_exp_f32 v225, v225\ns_waitcnt lgkmcnt(0)\n" \
  "v_smfmac_f32_32x32x32_bf16 v[240:255], v[204:207], v[216:223], %[idx]\nds_read_b128 v[188:191], %[lr0] offset:11264\nds_read_b128 v[176:179], %[lr0] offset:8192\nv_exp_f32 v226, v226\n" \
  "v_exp_f32 v227, v227\nv_exp_f32 v228, v228\nv_mfma_f32_32x32x16_bf16 v[208:223], v[192:195], v[172:175], 0\nds_read_b128 v[180:183], %[lr0] offset:9216\n" \
  "ds_read_b128 v[184:187], %[lr0] offset:10240\nv_exp_f32 v229, v229\nv_exp_f32 v230, v230\nv_exp_f32 v231, v231\n" \
  "v_mfma_f32_32x32x16_bf16 v[208:223], v[196:199], v[168:171], v[208:223]\ns_add_u32 %[ko], %[ko], 0x4000\ns_and_b32 %[ko], %[ko], 0x3ffff\ns_add_u32 m0, %[m], 65536\n" \
  "s_nop 0\nbuffer_load_dwordx4 %[l16], %[rkv], %[ko] offen offset:0 sc1 lds\nbuffer_load_dwordx4 %[l16], %[rkv], %[ko] offen offset:1024 sc1 lds\nv_exp_f32 v232, v232\n" \
  "v_exp_f32 v233, v233\nv_exp_f32 v234, v234\nv_exp_f32 v235, v235\nv_smfmac_f32_32x32x32_bf16 v[240:255], v[200:203], v[224:231], %[idx]\n" \
  "s_add_u32 %[t], %[wo], 0\nbuffer_load_dwordx4 v[0:3], %[l16], %[rw], %[t] offen offset:0 sc1\nbuffer_load_dwordx4 v[4:7], %[l16], %[rw], %[t] offen offset:1024 sc1\nv_exp_f32 v236, v236\n" \
  "v_exp_f32 v237, v237\nv_exp_f32 v238, v238\nv_exp_f32 v239, v239\ns_setprio 0\n" \
  "v_exp_f32 v208, v208\nv_exp_f32 v209, v209\ns_waitcnt lgkmcnt(0)\nv_smfmac_f32_32x32x32_bf16 v[240:255], v[188:191], v[232:239], %[idx]\n" \
  "ds_read_b128 v[204:207], %[lr0] offset:15360\nds_read_b128 v[192:195], %[lr0] offset:12288\nv_exp_f32 v210, v210\nv_exp_f32 v211, v211\n" \
  "v_exp_f32 v212, v212\nv_mfma_f32_32x32x16_bf16 v[224:239], v[176:179], v[172:175], 0\nds_read_b128 v[196:199], %[lr0] offset:13312\nds_read_b128 v[200:203], %[lr0] offset:14336\n" \
  "v_exp_f32 v213, v213\nv_exp_f32 v214, v214\nv_exp_f32 v215, v215\nv_mfma_f32_32x32x16_bf16 v[224:239], v[180:183], v[168:171], v[224:239]\n" \
  "buffer_load_dwordx4 %[l16], %[rkv], %[ko] offen offset:2048 sc1 lds\nbuffer_load_dwordx4 %[l16], %[rkv], %[ko] offen offset:3072 sc1 lds\nv_exp_f32 v216, v216\nv_exp_f32 v217, v217\n" \
  "v_exp_f32 v218, v218\nv_exp_f32 v219, v219\nv_smfmac_f32_32x32x32_bf16 v[240:255], v[184:187], v[208:215], %[idx]\nbuffer_load_dwordx4 v[8:11], %[l16], %[rw], %[t] offen offset:2048 sc1\n" \
  "v_exp_f32 v220, v220\nv_exp_f32 v221, v221\nv_exp_f32 v222, v222\nv_exp_f32 v223, v223\n" \
  "s_waitcnt vmcnt(7) lgkmcnt(0)\ns_barrier\ns_setprio 3\nv_exp_f32 v224, v224\n" \
  "v_exp_f32 v225, v225\nv_smfmac_f32_32x32x32_bf16 v[240:255], v[204:207], v[216:223], %[idx]\nds_read_b128 v[188:191], %[lr0] offset:35840\nds_read_b128 v[176:179], %[lr0] offset:32768\n" \
  "v_exp_f32 v226, v226\nv_exp_f32 v227, v227\nv_exp_f32 v228, v228\nv_mfma_f32_32x32x16_bf16 v[208:223], v[192:195], v[172:175], 0\n" \
  "ds_read_b128 v[180:183], %[lr0] offset:33792\nds_read_b128 v[184:187], %[lr0] offset:34816\nv_exp_f32 v229, v229\nv_exp_f32 v230, v230\n" \
  "v_exp_f32 v231, v231\nv_mfma_f32_32x32x16_bf16 v[208:223], v[196:199], v[168:171], v[208:223]\ns_add_u32 %[ko], %[ko], 0x4000\ns_and_b32 %[ko], %[ko], 0x3ffff\n" \
  "s_add_u32 m0, %[m], 0\ns_nop 0\nbuffer_load_dwordx4 %[l16], %[rkv], %[ko] offen offset:0 sc1 lds\nv_exp_f32 v232, v232\n" \
  "v_exp_f32 v233, v233\nv_exp_f32 v234, v234\nv_exp_f32 v235, v235\nv_smfmac_f32_32x32x32_bf16 v[240:255], v[200:203], v[224:231], %[idx]\n" \
  "buffer_load_dwordx4 v[12:15], %[l16], %[rw], %[t] offen offset:3072 sc1\ns_add_u32 %[t], %[wo], 4096\nbuffer_load_dwordx4 v[16:19], %[l16], %[rw], %[t] offen offset:0 sc1\nv_exp_f32 v236, v236\n" \
  "v_exp_f32 v237, v237\nv_exp_f32 v238, v238\nv_exp_f32 v239, v239\ns_setprio 2\n" \
  "v_exp_f32 v208, v208\nv_exp_f32 v209, v209\ns_waitcnt lgkmcnt(0)\nv_smfmac_f32_32x32x32_bf16 v[240:255], v[188:191], v[232:239], %[idx]\n" \
  "ds_read_b128 v[204:207], %[lr0] offset:39936\nds_read_b128 v[192:195], %[lr0] offset:36864\nv_exp_f32 v210, v210\nv_exp_f32 v211, v211\n" \
  "v_exp_f32 v212, v212\nv_mfma_f32_32x32x16_bf16 v[224:239], v[176:179], v[172:175], 0\nds_read_b128 v[196:199], %[lr0] offset:37888\nds_read_b128 v[200:203], %[lr0] offset:38912\n" \
  "v_exp_f32 v213, v213\nv_exp_f32 v214, v214\nv_exp_f32 v215, v215\nv_mfma_f32_32x32x16_bf16 v[224:239], v[180:183], v[168:171], v[224:239]\n" \
  "buffer_load_dwordx4 %[l16], %[rkv], %[ko] offen offset:1024 sc1 lds\nv_exp_f32 v216, v216\nv_exp_f32 v217, v217\nv_exp_f32 v218, v218\n" \
  "v_exp_f32 v219, v219\nv_smfmac_f32_32x32x32_bf16 v[240:255], v[184:187], v[208:215], %[idx]\nbuffer_load_dwordx4 v[20:23], %[l16], %[rw], %[t] offen offset:1024 sc1\nv_exp_f32 v220, v220\n" \
  "v_exp_f32 v221, v221\nv_exp_f32 v222, v222\nv_exp_f32 v223, v223\ns_setprio 1\n" \
  "v_exp_f32 v224, v224\nv_exp_f32 v225, v225\ns_waitcnt lgkmcnt(0)\nv_smfmac_f32_32x32x32_bf16 v[240:255], v[204:207], v[216:223], %[idx]\n" \
  "ds_read_b128 v[188:191], %[lr0] offset:44032\nds_read_b128 v[176:179], %[lr0] offset:40960\nv_exp_f32 v226, v226\nv_exp_f32 v227, v227\n" \
  "v_exp_f32 v228, v228\nv_mfma_f32_32x32x16_bf16 v[208:223], v[192:195], v[172:175], 0\nds_read_b128 v[180:183], %[lr0] offset:41984\nds_read_b128 v[184:187], %[lr0] offset:43008\n" \
  "v_exp_f32 v229, v229\nv_exp_f32 v230, v230\nv_exp_f32 v231, v231\nv_mfma_f32_32x32x16_bf16 v[208:223], v[196:199], v[168:171], v[208:223]\n" \
  "buffer_load_dwordx4 %[l16], %[rkv], %[ko] offen offset:2048 sc1 lds\nv_exp_f32 v232, v232\nv_exp_f32 v233, v233\nv_exp_f32 v234, v234\n" \
  "v_exp_f32 v235, v235\nv_smfmac_f32_32x32x32_bf16 v[240:255], v[200:203], v[224:231], %[idx]\nbuffer_load_dwordx4 v[24:27], %[l16], %[rw], %[t] offen offset:2048 sc1\nv_exp_f32 v236, v236\n" \
  "v_exp_f32 v237, v237\nv_exp_f32 v238, v238\nv_exp_f32 v239, v239\ns_setprio 0\n" \
  "v_exp_f32 v208, v208\nv_exp_f32 v209, v209\ns_waitcnt lgkmcnt(0)\nv_smfmac_f32_32x32x32_bf16 v[240:255], v[188:191], v[232:239], %[idx]\n" \
  "ds_read_b128 v[204:207], %[lr0] offset:48128\nds_read_b128 v[192:195], %[lr0] offset:45056\nv_exp_f32 v210, v210\nv_exp_f32 v211, v211\n" \
  "v_exp_f32 v212, v212\nv_mfma_f32_32x32x16_bf16 v[224:239], v[176:179], v[172:175], 0\nds_read_b128 v[196:199], %[lr0] offset:46080\nds_read_b128 v[200:203], %[lr0] offset:47104\n" \
  "v_exp_f32 v213, v213\nv_exp_f32 v214, v214\nv_exp_f32 v215, v215\nv_mfma_f32_32x32x16_bf16 v[224:239], v[180:183], v[168:171], v[224:239]\n" \
  "buffer_load_dwordx4 %[l16], %[rkv], %[ko] offen offset:3072 sc1 lds\nv_exp_f32 v216, v216\nv_exp_f32 v217, v217\nv_exp_f32 v218, v218\n" \
  "v_exp_f32 v219, v219\nv_smfmac_f32_32x32x32_bf16 v[240:255], v[184:187], v[208:215], %[idx]\nbuffer_load_dwordx4 v[28:31], %[l16], %[rw], %[t] offen offset:3072 sc1\ns_add_u32 %[t], %[wo], 8192\n" \
  "buffer_load_dwordx4 v[32:35], %[l16], %[rw], %[t] offen offset:0 sc1\nv_exp_f32 v220, v220\nv_exp_f32 v221, v221\nv_exp_f32 v222, v222\n" \
  "v_exp_f32 v223, v223\ns_waitcnt vmcnt(11) lgkmcnt(0)\ns_barrier\ns_setprio 3\n" \
  "v_exp_f32 v224, v224\nv_exp_f32 v225, v225\nv_smfmac_f32_32x32x32_bf16 v[240:255], v[204:207], v[216:223], %[idx]\nds_read_b128 v[188:191], %[lr2] offset:3072\n" \
  "ds_read_b128 v[176:179], %[lr2] offset:0\nv_exp_f32 v226, v226\nv_exp_f32 v227, v227\nv_exp_f32 v228, v228\n" \
  "v_mfma_f32_32x32x16_bf16 v[208:223], v[192:195], v[172:175], 0\nds_read_b128 v[180:183], %[lr2] offset:1024\nds_read_b128 v[184:187], %[lr2] offset:2048\nv_exp_f32 v229, v229\n" \
  "v_exp_f32 v230, v230\nv_exp_f32 v231, v231\nv_mfma_f32_32x32x16_bf16 v[208:223], v[196:199], v[168:171], v[208:223]\ns_add_u32 %[ko], %[ko], 0x4000\n" \
  "s_and_b32 %[ko], %[ko], 0x3ffff\ns_add_u32 m0, %[m], 32768\ns_nop 0\nbuffer_load_dwordx4 %[l16], %[rkv], %[ko] offen offset:0 sc1 lds\n" \
  "v_exp_f32 v232, v232\nv_exp_f32 v233, v233\nv_exp_f32 v234, v234\nv_exp_f32 v235, v235\n" \
  "v_smfmac_f32_32x32x32_bf16 v[240:255], v[200:203], v[224:231], %[idx]\nbuffer_load_dwordx4 v[36:39], %[l16], %[rw], %[t] offen offset:1024 sc1\nv_exp_f32 v236, v236\nv_exp_f32 v237, v237\n" \
  "v_exp_f32 v238, v238\nv_exp_f32 v239, v239\ns_setprio 2\nv_exp_f32 v208, v208\n" \
  "v_exp_f32 v209, v209\ns_waitcnt lgkmcnt(0)\nv_smfmac_f32_32x32x32_bf16 v[240:255], v[188:191], v[232:239], %[idx]\nds_read_b128 v[204:207], %[lr2] offset:7168\n" \
  "ds_read_b128 v[192:195], %[lr2] offset:4096\nv_exp_f32 v210, v210\nv_exp_f32 v211, v211\nv_exp_f32 v212, v212\n" \
  "v_mfma_f32_32x32x16_bf16 v[224:239], v[176:179], v[172:175], 0\nds_read_b128 v[196:199], %[lr2] offset:5120\nds_read_b128 v[200:203], %[lr2] offset:6144\nv_exp_f32 v213, v213\n" \
  "v_exp_f32 v214, v214\nv_exp_f32 v215, v215\nv_mfma_f32_32x32x16_bf16 v[224:239], v[180:183], v[168:171], v[224:239]\nbuffer_load_dwordx4 %[l16], %[rkv], %[ko] offen offset:1024 sc1 lds\n" \
  "v_exp_f32 v216, v216\nv_exp_f32 v217, v217\nv_exp_f32 v218, v218\nv_exp_f32 v219, v219\n" \
  "v_smfmac_f32_32x32x32_bf16 v[240:255], v[184:187], v[208:215], %[idx]\nbuffer_load_dwordx4 v[40:43], %[l16], %[rw], %[t] offen offset:2048 sc1\nbuffer_load_dwordx4 v[44:47], %[l16], %[rw], %[t] offen offset:3072 sc1\nv_exp_f32 v220, v220\n" \
  "v_exp_f32 v221, v221\nv_exp_f32 v222, v222\nv_exp_f32 v223, v223\ns_setprio 1\n" \
  "v_exp_f32 v224, v224\nv_exp_f32 v225, v225\ns_waitcnt lgkmcnt(0)\nv_smfmac_f32_32x32x32_bf16 v[240:255], v[204:207], v[216:223], %[idx]\n" \
  "ds_read_b128 v[188:191], %[lr2] offset:11264\nds_read_b128 v[176:179], %[lr2] offset:8192\nv_exp_f32 v226, v226\nv_exp_f32 v227, v227\n" \
  "v_exp_f32 v228, v228\nv_mfma_f32_32x32x16_bf16 v[208:223], v[192:195], v[172:175], 0\nds_read_b128 v[180:183], %[lr2] offset:9216\nds_read_b128 v[184:187], %[lr2] offset:10240\n" \
  "v_exp_f32 v229, v229\nv_exp_f32 v230, v230\nv_exp_f32 v231, v231\nv_mfma_f32_32x32x16_bf16 v[208:223], v[196:199], v[168:171], v[208:223]\n" \
  "buffer_load_dwordx4 %[l16], %[rkv], %[ko] offen offset:2048 sc1 lds\nv_exp_f32 v232, v232\nv_exp_f32 v233, v233\nv_exp_f32 v234, v234\n" \
  "v_exp_f32 v235, v235\nv_smfmac_f32_32x32x32_bf16 v[240:255], v[200:203], v[224:231], %[idx]\ns_add_u32 %[t], %[wo], 12288\nbuffer_load_dwordx4 v[48:51], %[l16], %[rw], %[t] offen offset:0 sc1\n" \
  "v_exp_f32 v236, v236\nv_exp_f32 v237, v237\nv_exp_f32 v238, v238\nv_exp_f32 v239, v239\n" \
  "s_setprio 0\nv_exp_f32 v208, v208\nv_exp_f32 v209, v209\ns_waitcnt lgkmcnt(0)\n" \
  "v_smfmac_f32_32x32x32_bf16 v[240:255], v[188:191], v[232:239], %[idx]\nds_read_b128 v[204:207], %[lr2] offset:15360\nds_read_b128 v[192:195], %[lr2] offset:12288\nv_exp_f32 v210, v210\n" \
  "v_exp_f32 v211, v211\nv_exp_f32 v212, v212\nv_mfma_f32_32x32x16_bf16 v[224:239], v[176:179], v[172:175], 0\nds_read_b128 v[196:199], %[lr2] offset:13312\n" \
  "ds_read_b128 v[200:203], %[lr2] offset:14336\nv_exp_f32 v213, v213\nv_exp_f32 v214, v214\nv_exp_f32 v215, v215\n" \
  "v_mfma_f32_32x32x16_bf16 v[224:239], v[180:183], v[168:171], v[224:239]\nbuffer_load_dwordx4 %[l16], %[rkv], %[ko] offen offset:3072 sc1 lds\nv_exp_f32 v216, v216\nv_exp_f32 v217, v217\n" \
  "v_exp_f32 v218, v218\nv_exp_f32 v219, v219\nv_smfmac_f32_32x32x32_bf16 v[240:255], v[184:187], v[208:215], %[idx]\nbuffer_load_dwordx4 v[52:55], %[l16], %[rw], %[t] offen offset:1024 sc1\n" \
  "v_exp_f32 v220, v220\nv_exp_f32 v221, v221\nv_exp_f32 v222, v222\nv_exp_f32 v223, v223\n" \
  "s_waitcnt vmcnt(11) lgkmcnt(0)\ns_barrier\ns_setprio 3\nv_exp_f32 v224, v224\n" \
  "v_exp_f32 v225, v225\nv_smfmac_f32_32x32x32_bf16 v[240:255], v[204:207], v[216:223], %[idx]\nds_read_b128 v[188:191], %[lr0] offset:3072\nds_read_b128 v[176:179], %[lr0] offset:0\n" \
  "v_exp_f32 v226, v226\nv_exp_f32 v227, v227\nv_exp_f32 v228, v228\nv_mfma_f32_32x32x16_bf16 v[208:223], v[192:195], v[172:175], 0\n" \
  "ds_read_b128 v[180:183], %[lr0] offset:1024\nds_read_b128 v[184:187], %[lr0] offset:2048\nv_exp_f32 v229, v229\nv_exp_f32 v230, v230\n" \
  "v_exp_f32 v231, v231\nv_mfma_f32_32x32x16_bf16 v[208:223], v[196:199], v[168:171], v[208:223]\ns_add_u32 %[ko], %[ko], 0x4000\ns_and_b32 %[ko], %[ko], 0x3ffff\n" \
  "s_add_u32 m0, %[m], 65536\ns_nop 0\nbuffer_load_dwordx4 %[l16], %[rkv], %[ko] offen offset:0 sc1 lds\nv_exp_f32 v232, v232\n" \
  "v_exp_f32 v233, v233\nv_exp_f32 v234, v234\nv_exp_f32 v235, v235\nv_smfmac_f32_32x32x32_bf16 v[240:255], v[200:203], v[224:231], %[idx]\n" \
  "buffer_load_dwordx4 v[56:59], %[l16], %[rw], %[t] offen offset:2048 sc1\nbuffer_load_dwordx4 v[60:63], %[l16], %[rw], %[t] offen offset:3072 sc1\nv_exp_f32 v236, v236\nv_exp_f32 v237, v237\n" \
  "v_exp_f32 v238, v238\nv_exp_f32 v239, v239\ns_setprio 2\nv_exp_f32 v208, v208\n" \
  "v_exp_f32 v209, v209\ns_waitcnt lgkmcnt(0)\nv_smfmac_f32_32x32x32_bf16 v[240:255], v[188:191], v[232:239], %[idx]\nds_read_b128 v[204:207], %[lr0] offset:7168\n" \
  "ds_read_b128 v[192:195], %[lr0] offset:4096\nv_exp_f32 v210, v210\nv_exp_f32 v211, v211\nv_exp_f32 v212, v212\n" \
  "v_mfma_f32_32x32x16_bf16 v[224:239], v[176:179], v[172:175], 0\nds_read_b128 v[196:199], %[lr0] offset:5120\nds_read_b128 v[200:203], %[lr0] offset:6144\nv_exp_f32 v213, v213\n" \
  "v_exp_f32 v214, v214\nv_exp_f32 v215, v215\nv_mfma_f32_32x32x16_bf16 v[224:239], v[180:183], v[168:171], v[224:239]\nbuffer_load_dwordx4 %[l16], %[rkv], %[ko] offen offset:1024 sc1 lds\n" \
  "v_exp_f32 v216, v216\nv_exp_f32 v217, v217\nv_exp_f32 v218, v218\nv_exp_f32 v219, v219\n" \
  "v_smfmac_f32_32x32x32_bf16 v[240:255], v[184:187], v[208:215], %[idx]\ns_add_u32 %[t], %[w1], 0\nbuffer_load_dwordx4 v[64:67], %[l16], %[rw], %[t] offen offset:0 sc1\nv_exp_f32 v220, v220\n" \
  "v_exp_f32 v221, v221\nv_exp_f32 v222, v222\nv_exp_f32 v223, v223\ns_setprio 1\n" \
  "v_exp_f32 v224, v224\nv_exp_f32 v225, v225\ns_waitcnt lgkmcnt(0)\nv_smfmac_f32_32x32x32_bf16 v[240:255], v[204:207], v[216:223], %[idx]\n" \
  "ds_read_b128 v[188:191], %[lr0] offset:11264\nds_read_b128 v[176:179], %[lr0] offset:8192\nv_exp_f32 v226, v226\nv_exp_f32 v227, v227\n" \
  "v_exp_f32 v228, v228\nv_mfma_f32_32x32x16_bf16 v[208:223], v[192:195], v[172:175], 0\nds_read_b128 v[180:183], %[lr0] offset:9216\nds_read_b128 v[184:187], %[lr0] offset:10240\n" \
  "v_exp_f32 v229, v229\nv_exp_f32 v230, v230\nv_exp_f32 v231, v231\nv_mfma_f32_32x32x16_bf16 v[208:223], v[196:199], v[168:171], v[208:223]\n" \
  "buffer_load_dwordx4 %[l16], %[rkv], %[ko] offen offset:2048 sc1 lds\nv_exp_f32 v232, v232\nv_exp_f32 v233, v233\nv_exp_f32 v234, v234\n" \
  "v_exp_f32 v235, v235\nv_smfmac_f32_32x32x32_bf16 v[240:255], v[200:203], v[224:231], %[idx]\nbuffer_load_dwordx4 v[68:71], %[l16], %[rw], %[t] offen offset:1024 sc1\nv_exp_f32 v236, v236\n" \
  "v_exp_f32 v237, v237\nv_exp_f32 v238, v238\nv_exp_f32 v239, v239\ns_setprio 0\n" \
  "v_exp_f32 v208, v208\nv_exp_f32 v209, v209\ns_waitcnt lgkmcnt(0)\nv_smfmac_f32_32x32x32_bf16 v[240:255], v[188:191], v[232:239], %[idx]\n" \
  "ds_read_b128 v[204:207], %[lr0] offset:15360\nds_read_b128 v[192:195], %[lr0] offset:12288\nv_exp_f32 v210, v210\nv_exp_f32 v211, v211\n" \
  "v_exp_f32 v212, v212\nv_mfma_f32_32x32x16_bf16 v[224:239], v[176:179], v[172:175], 0\nds_read_b128 v[196:199], %[lr0] offset:13312\nds_read_b128 v[200:203], %[lr0] offset:14336\n" \
  "v_exp_f32 v213, v213\nv_exp_f32 v214, v214\nv_exp_f32 v215, v215\nv_mfma_f32_32x32x16_bf16 v[224:239], v[180:183], v[168:171], v[224:239]\n" \
  "buffer_load_dwordx4 %[l16], %[rkv], %[ko] offen offset:3072 sc1 lds\nv_exp_f32 v216, v216\nv_exp_f32 v217, v217\nv_exp_f32 v218, v218\n" \
  "v_exp_f32 v219, v219\nv_smfmac_f32_32x32x32_bf16 v[240:255], v[184:187], v[208:215], %[idx]\nbuffer_load_dwordx4 v[72:75], %[l16], %[rw], %[t] offen offset:2048 sc1\nbuffer_load_dwordx4 v[76:79], %[l16], %[rw], %[t] offen offset:3072 sc1\n" \
  "v_exp_f32 v220, v220\nv_exp_f32 v221, v221\nv_exp_f32 v222, v222\nv_exp_f32 v223, v223\n" \
  "s_waitcnt vmcnt(11) lgkmcnt(0)\ns_barrier\ns_setprio 3\nv_exp_f32 v224, v224\n" \
  "v_exp_f32 v225, v225\nv_smfmac_f32_32x32x32_bf16 v[240:255], v[204:207], v[216:223], %[idx]\nds_read_b128 v[188:191], %[lr0] offset:35840\nds_read_b128 v[176:179], %[lr0] offset:32768\n" \
  "v_exp_f32 v226, v226\nv_exp_f32 v227, v227\nv_exp_f32 v228, v228\nv_mfma_f32_32x32x16_bf16 v[208:223], v[192:195], v[172:175], 0\n" \
  "ds_read_b128 v[180:183], %[lr0] offset:33792\nds_read_b128 v[184:187], %[lr0] offset:34816\nv_exp_f32 v229, v229\nv_exp_f32 v230, v230\n" \
  "v_exp_f32 v231, v231\nv_mfma_f32_32x32x16_bf16 v[208:223], v[196:199], v[168:171], v[208:223]\ns_add_u32 %[ko], %[ko], 0x4000\ns_and_b32 %[ko], %[ko], 0x3ffff\n" \
  "s_add_u32 m0, %[m], 0\ns_nop 0\nbuffer_load_dwordx4 %[l16], %[rkv], %[ko] offen offset:0 sc1 lds\nv_exp_f32 v232, v232\n" \
  "v_exp_f32 v233, v233\nv_exp_f32 v234, v234\nv_exp_f32 v235, v235\nv_smfmac_f32_32x32x32_bf16 v[240:255], v[200:203], v[224:231], %[idx]\n" \
  "s_add_u32 %[t], %[w1], 4096\nbuffer_load_dwordx4 v[80:83], %[l16], %[rw], %[t] offen offset:0 sc1\nv_exp_f32 v236, v236\nv_exp_f32 v237, v237\n" \
  "v_exp_f32 v238, v238\nv_exp_f32 v239, v239\ns_setprio 2\nv_exp_f32 v208, v208\n" \
  "v_exp_f32 v209, v209\ns_waitcnt lgkmcnt(0)\nv_smfmac_f32_32x32x32_bf16 v[240:255], v[188:191], v[232:239], %[idx]\nds_read_b128 v[204:207], %[lr0] offset:39936\n" \
  "ds_read_b128 v[192:195], %[lr0] offset:36864\nv_exp_f32 v210, v210\nv_exp_f32 v211, v211\nv_exp_f32 v212, v212\n" \
  "v_mfma_f32_32x32x16_bf16 v[224:239], v[176:179], v[172:175], 0\nds_read_b128 v[196:199], %[lr0] offset:37888\nds_read_b128 v[200:203], %[lr0] offset:38912\nv_exp_f32 v213, v213\n" \
  "v_exp_f32 v214, v214\nv_exp_f32 v215, v215\nv_mfma_f32_32x32x16_bf16 v[224:239], v[180:183], v[168:171], v[224:239]\nbuffer_load_dwordx4 %[l16], %[rkv], %[ko] offen offset:1024 sc1 lds\n" \
  "v_exp_f32 v216, v216\nv_exp_f32 v217, v217\nv_exp_f32 v218, v218\nv_exp_f32 v219, v219\n" \
  "v_smfmac_f32_32x32x32_bf16 v[240:255], v[184:187], v[208:215], %[idx]\nbuffer_load_dwordx4 v[84:87], %[l16], %[rw], %[t] offen offset:1024 sc1\nbuffer_load_dwordx4 v[88:91], %[l16], %[rw], %[t] offen offset:2048 sc1\nv_exp_f32 v220, v220\n" \
  "v_exp_f32 v221, v221\nv_exp_f32 v222, v222\nv_exp_f32 v223, v223\ns_setprio 1\n" \
  "v_exp_f32 v224, v224\nv_exp_f32 v225, v225\ns_waitcnt lgkmcnt(0)\nv_smfmac_f32_32x32x32_bf16 v[240:255], v[204:207], v[216:223], %[idx]\n" \
  "ds_read_b128 v[188:191], %[lr0] offset:44032\nds_read_b128 v[176:179], %[lr0] offset:40960\nv_exp_f32 v226, v226\nv_exp_f32 v227, v227\n" \
  "v_exp_f32 v228, v228\nv_mfma_f32_32x32x16_bf16 v[208:223], v[192:195], v[172:175], 0\nds_read_b128 v[180:183], %[lr0] offset:41984\nds_read_b128 v[184:187], %[lr0] offset:43008\n" \
  "v_exp_f32 v229, v229\nv_exp_f32 v230, v230\nv_exp_f32 v231, v231\nv_mfma_f32_32x32x16_bf16 v[208:223], v[196:199], v[168:171], v[208:223]\n" \
  "buffer_load_dwordx4 %[l16], %[rkv], %[ko] offen offset:2048 sc1 lds\nv_exp_f32 v232, v232\nv_exp_f32 v233, v233\nv_exp_f32 v234, v234\n" \
  "v_exp_f32 v235, v235\nv_smfmac_f32_32x32x32_bf16 v[240:255], v[200:203], v[224:231], %[idx]\nbuffer_load_dwordx4 v[92:95], %[l16], %[rw], %[t] offen offset:3072 sc1\nv_exp_f32 v236, v236\n" \
  "v_exp_f32 v237, v237\nv_exp_f32 v238, v238\nv_exp_f32 v239, v239\ns_setprio 0\n" \
  "v_exp_f32 v208, v208\nv_exp_f32 v209, v209\ns_waitcnt lgkmcnt(0)\nv_smfmac_f32_32x32x32_bf16 v[240:255], v[188:191], v[232:239], %[idx]\n" \
  "ds_read_b128 v[204:207], %[lr0] offset:48128\nds_read_b128 v[192:195], %[lr0] offset:45056\nv_exp_f32 v210, v210\nv_exp_f32 v211, v211\n" \
  "v_exp_f32 v212, v212\nv_mfma_f32_32x32x16_bf16 v[224:239], v[176:179], v[172:175], 0\nds_read_b128 v[196:199], %[lr0] offset:46080\nds_read_b128 v[200:203], %[lr0] offset:47104\n" \
  "v_exp_f32 v213, v213\nv_exp_f32 v214, v214\nv_exp_f32 v215, v215\nv_mfma_f32_32x32x16_bf16 v[224:239], v[180:183], v[168:171], v[224:239]\n" \
  "buffer_load_dwordx4 %[l16], %[rkv], %[ko] offen offset:3072 sc1 lds\nv_exp_f32 v216, v216\nv_exp_f32 v217, v217\nv_exp_f32 v218, v218\n" \
  "v_exp_f32 v219, v219\nv_smfmac_f32_32x32x32_bf16 v[240:255], v[184:187], v[208:215], %[idx]\ns_add_u32 %[t], %[w1], 8192\nbuffer_load_dwordx4 v[96:99], %[l16], %[rw], %[t] offen offset:0 sc1\n" \
  "v_exp_f32 v220, v220\nv_exp_f32 v221, v221\nv_exp_f32 v222, v222\nv_exp_f32 v223, v223\n" \
  "s_waitcnt vmcnt(11) lgkmcnt(0)\ns_barrier\ns_setprio 3\nv_exp_f32 v224, v224\n" \
  "v_exp_f32 v225, v225\nv_smfmac_f32_32x32x32_bf16 v[240:255], v[204:207], v[216:223], %[idx]\nds_read_b128 v[188:191], %[lr2] offset:3072\nds_read_b128 v[176:179], %[lr2] offset:0\n" \
  "v_exp_f32 v226, v226\nv_exp_f32 v227, v227\nv_exp_f32 v228, v228\nv_mfma_f32_32x32x16_bf16 v[208:223], v[192:195], v[172:175], 0\n" \
  "ds_read_b128 v[180:183], %[lr2] offset:1024\nds_read_b128 v[184:187], %[lr2] offset:2048\nv_exp_f32 v229, v229\nv_exp_f32 v230, v230\n" \
  "v_exp_f32 v231, v231\nv_mfma_f32_32x32x16_bf16 v[208:223], v[196:199], v[168:171], v[208:223]\ns_add_u32 %[ko], %[ko], 0x4000\ns_and_b32 %[ko], %[ko], 0x3ffff\n" \
  "s_add_u32 m0, %[m], 32768\ns_nop 0\nbuffer_load_dwordx4 %[l16], %[rkv], %[ko] offen offset:0 sc1 lds\nv_exp_f32 v232, v232\n" \
  "v_exp_f32 v233, v233\nv_exp_f32 v234, v234\nv_exp_f32 v235, v235\nv_smfmac_f32_32x32x32_bf16 v[240:255], v[200:203], v[224:231], %[idx]\n" \
  "buffer_load_dwordx4 v[100:103], %[l16], %[rw], %[t] offen offset:1024 sc1\nbuffer_load_dwordx4 v[104:107], %[l16], %[rw], %[t] offen offset:2048 sc1\nv_exp_f32 v236, v236\nv_exp_f32 v237, v237\n" \
  "v_exp_f32 v238, v238\nv_exp_f32 v239, v239\ns_setprio 2\nv_exp_f32 v208, v208\n" \
  "v_exp_f32 v209, v209\ns_waitcnt lgkmcnt(0)\nv_smfmac_f32_32x32x32_bf16 v[240:255], v[188:191], v[232:239], %[idx]\nds_read_b128 v[204:207], %[lr2] offset:7168\n" \
  "ds_read_b128 v[192:195], %[lr2] offset:4096\nv_exp_f32 v210, v210\nv_exp_f32 v211, v211\nv_exp_f32 v212, v212\n" \
  "v_mfma_f32_32x32x16_bf16 v[224:239], v[176:179], v[172:175], 0\nds_read_b128 v[196:199], %[lr2] offset:5120\nds_read_b128 v[200:203], %[lr2] offset:6144\nv_exp_f32 v213, v213\n" \
  "v_exp_f32 v214, v214\nv_exp_f32 v215, v215\nv_mfma_f32_32x32x16_bf16 v[224:239], v[180:183], v[168:171], v[224:239]\nbuffer_load_dwordx4 %[l16], %[rkv], %[ko] offen offset:1024 sc1 lds\n" \
  "v_exp_f32 v216, v216\nv_exp_f32 v217, v217\nv_exp_f32 v218, v218\nv_exp_f32 v219, v219\n" \
  "v_smfmac_f32_32x32x32_bf16 v[240:255], v[184:187], v[208:215], %[idx]\nbuffer_load_dwordx4 v[108:111], %[l16], %[rw], %[t] offen offset:3072 sc1\nv_exp_f32 v220, v220\nv_exp_f32 v221, v221\n" \
  "v_exp_f32 v222, v222\nv_exp_f32 v223, v223\ns_setprio 1\nv_exp_f32 v224, v224\n" \
  "v_exp_f32 v225, v225\ns_waitcnt lgkmcnt(0)\nv_smfmac_f32_32x32x32_bf16 v[240:255], v[204:207], v[216:223], %[idx]\nds_read_b128 v[188:191], %[lr2] offset:11264\n" \
  "ds_read_b128 v[176:179], %[lr2] offset:8192\nv_exp_f32 v226, v226\nv_exp_f32 v227, v227\nv_exp_f32 v228, v228\n" \
  "v_mfma_f32_32x32x16_bf16 v[208:223], v[192:195], v[172:175], 0\nds_read_b128 v[180:183], %[lr2] offset:9216\nds_read_b128 v[184:187], %[lr2] offset:10240\nv_exp_f32 v229, v229\n" \
  "v_exp_f32 v230, v230\nv_exp_f32 v231, v231\nv_mfma_f32_32x32x16_bf16 v[208:223], v[196:199], v[168:171], v[208:223]\nbuffer_load_dwordx4 %[l16], %[rkv], %[ko] offen offset:2048 sc1 lds\n" \
  "v_exp_f32 v232, v232\nv_exp_f32 v233, v233\nv_exp_f32 v234, v234\nv_exp_f32 v235, v235\n" \
  "v_smfmac_f32_32x32x32_bf16 v[240:255], v[200:203], v[224:231], %[idx]\ns_add_u32 %[t], %[w1], 12288\nbuffer_load_dwordx4 v[112:115], %[l16], %[rw], %[t] offen offset:0 sc1\nbuffer_load_dwordx4 v[116:119], %[l16], %[rw], %[t] offen offset:1024 sc1\n" \
  "v_exp_f32 v236, v236\nv_exp_f32 v237, v237\nv_exp_f32 v238, v238\nv_exp_f32 v239, v239\n" \
  "s_setprio 0\nv_exp_f32 v208, v208\nv_exp_f32 v209, v209\ns_waitcnt lgkmcnt(0)\n" \
  "v_smfmac_f32_32x32x32_bf16 v[240:255], v[188:191], v[232:239], %[idx]\nds_read_b128 v[204:207], %[lr2] offset:15360\nds_read_b128 v[192:195], %[lr2] offset:12288\nv_exp_f32 v210, v210\n" \
  "v_exp_f32 v211, v211\nv_exp_f32 v212, v212\nv_mfma_f32_32x32x16_bf16 v[224:239], v[176:179], v[172:175], 0\nds_read_b128 v[196:199], %[lr2] offset:13312\n" \
  "ds_read_b128 v[200:203], %[lr2] offset:14336\nv_exp_f32 v213, v213\nv_exp_f32 v214, v214\nv_exp_f32 v215, v215\n" \
  "v_mfma_f32_32x32x16_bf16 v[224:239], v[180:183], v[168:171], v[224:239]\nbuffer_load_dwordx4 %[l16], %[rkv], %[ko] offen offset:3072 sc1 lds\nv_exp_f32 v216, v216\nv_exp_f32 v217, v217\n" \
  "v_exp_f32 v218, v218\nv_exp_f32 v219, v219\nv_smfmac_f32_32x32x32_bf16 v[240:255], v[184:187], v[208:215], %[idx]\nbuffer_load_dwordx4 v[120:123], %[l16], %[rw], %[t] offen offset:2048 sc1\n" \
  "v_exp_f32 v220, v220\nv_exp_f32 v221, v221\nv_exp_f32 v222, v222\nv_exp_f32 v223, v223\n" \
  "s_waitcnt vmcnt(11) lgkmcnt(0)\ns_barrier\ns_setprio 3\nv_exp_f32 v224, v224\n" \
  "v_exp_f32 v225, v225\nv_smfmac_f32_32x32x32_bf16 v[240:255], v[204:207], v[216:223], %[idx]\nds_read_b128 v[188:191], %[lr0] offset:3072\nds_read_b128 v[176:179], %[lr0] offset:0\n" \
  "v_exp_f32 v226, v226\nv_exp_f32 v227, v227\nv_exp_f32 v228, v228\nv_mfma_f32_32x32x16_bf16 v[208:223], v[192:195], v[172:175], 0\n" \
  "ds_read_b128 v[180:183], %[lr0] offset:1024\nds_read_b128 v[184:187], %[lr0] offset:2048\nv_exp_f32 v229, v229\nv_exp_f32 v230, v230\n" \
  "v_exp_f32 v231, v231\nv_mfma_f32_32x32x16_bf16 v[208:223], v[196:199], v[168:171], v[208:223]\nv_exp_f32 v232, v232\nv_exp_f32 v233, v233\n" \
  "v_exp_f32 v234, v234\nv_exp_f32 v235, v235\nv_smfmac_f32_32x32x32_bf16 v[240:255], v[200:203], v[224:231], %[idx]\nbuffer_load_dwordx4 v[124:127], %[l16], %[rw], %[t] offen offset:3072 sc1\n" \
  "s_nop 0\nv_exp_f32 v236, v236\nv_exp_f32 v237, v237\nv_exp_f32 v238, v238\n" \
  "v_exp_f32 v239, v239\ns_setprio 2\nv_exp_f32 v208, v208\nv_exp_f32 v209, v209\n" \
  "s_waitcnt lgkmcnt(0)\nv_smfmac_f32_32x32x32_bf16 v[240:255], v[188:191], v[232:239], %[idx]\nds_read_b128 v[204:207], %[lr0] offset:7168\nds_read_b128 v[192:195], %[lr0] offset:4096\n" \
  "v_exp_f32 v210, v210\nv_exp_f32 v211, v211\nv_exp_f32 v212, v212\nv_mfma_f32_32x32x16_bf16 v[224:239], v[176:179], v[172:175], 0\n" \
  "ds_read_b128 v[196:199], %[lr0] offset:5120\nds_read_b128 v[200:203], %[lr0] offset:6144\nv_exp_f32 v213, v213\nv_exp_f32 v214, v214\n" \
  "v_exp_f32 v215, v215\nv_mfma_f32_32x32x16_bf16 v[224:239], v[180:183], v[168:171], v[224:239]\nv_exp_f32 v216, v216\nv_exp_f32 v217, v217\n" \
  "v_exp_f32 v218, v218\nv_exp_f32 v219, v219\nv_smfmac_f32_32x32x32_bf16 v[240:255], v[184:187], v[208:215], %[idx]\ns_add_u32 %[t], %[w1], 16384\n" \
  "buffer_load_dwordx4 v[128:131], %[l16], %[rw], %[t] offen offset:0 sc1\nbuffer_load_dwordx4 v[132:135], %[l16], %[rw], %[t] offen offset:1024 sc1\nv_exp_f32 v220, v220\nv_exp_f32 v221, v221\n" \
  "v_exp_f32 v222, v222\nv_exp_f32 v223, v223\ns_setprio 1\nv_exp_f32 v224, v224\n" \
  "v_exp_f32 v225, v225\ns_waitcnt lgkmcnt(0)\nv_smfmac_f32_32x32x32_bf16 v[240:255], v[204:207], v[216:223], %[idx]\nds_read_b128 v[188:191], %[lr0] offset:11264\n" \
  "ds_read_b128 v[176:179], %[lr0] offset:8192\nv_exp_f32 v226, v226\nv_exp_f32 v227, v227\nv_exp_f32 v228, v228\n" \
  "v_mfma_f32_32x32x16_bf16 v[208:223], v[192:195], v[172:175], 0\nds_read_b128 v[180:183], %[lr0] offset:9216\nds_read_b128 v[184:187], %[lr0] offset:10240\nv_exp_f32 v229, v229\n" \
  "v_exp_f32 v230, v230\nv_exp_f32 v231, v231\nv_mfma_f32_32x32x16_bf16 v[208:223], v[196:199], v[168:171], v[208:223]\nv_exp_f32 v232, v232\n" \
  "v_exp_f32 v233, v233\nv_exp_f32 v234, v234\nv_exp_f32 v235, v235\nv_smfmac_f32_32x32x32_bf16 v[240:255], v[200:203], v[224:231], %[idx]\n" \
  "buffer_load_dwordx4 v[136:139], %[l16], %[rw], %[t] offen offset:2048 sc1\ns_nop 0\nv_exp_f32 v236, v236\nv_exp_f32 v237, v237\n" \
  "v_exp_f32 v238, v238\nv_exp_f32 v239, v239\ns_setprio 0\nv_exp_f32 v208, v208\n" \
  "v_exp_f32 v209, v209\ns_waitcnt lgkmcnt(0)\nv_smfmac_f32_32x32x32_bf16 v[240:255], v[188:191], v[232:239], %[idx]\nds_read_b128 v[204:207], %[lr0] offset:15360\n" \
  "ds_read_b128 v[192:195], %[lr0] offset:12288\nv_exp_f32 v210, v210\nv_exp_f32 v211, v211\nv_exp_f32 v212, v212\n" \
  "v_mfma_f32_32x32x16_bf16 v[224:239], v[176:179], v[172:175], 0\nds_read_b128 v[196:199], %[lr0] offset:13312\nds_read_b128 v[200:203], %[lr0] offset:14336\nv_exp_f32 v213, v213\n" \
  "v_exp_f32 v214, v214\nv_exp_f32 v215, v215\nv_mfma_f32_32x32x16_bf16 v[224:239], v[180:183], v[168:171], v[224:239]\nv_exp_f32 v216, v216\n" \
  "v_exp_f32 v217, v217\nv_exp_f32 v218, v218\nv_exp_f32 v219, v219\nv_smfmac_f32_32x32x32_bf16 v[240:255], v[184:187], v[208:215], %[idx]\n" \
  "buffer_load_dwordx4 v[140:143], %[l16], %[rw], %[t] offen offset:3072 sc1\ns_nop 0\nv_exp_f32 v220, v220\nv_exp_f32 v221, v221\n" \
  "v_exp_f32 v222, v222\nv_exp_f32 v223, v223\ns_waitcnt vmcnt(6) lgkmcnt(0)\ns_barrier\n" \
  "s_setprio 3\nv_exp_f32 v224, v224\nv_exp_f32 v225, v225\nv_smfmac_f32_32x32x32_bf16 v[240:255], v[204:207], v[216:223], %[idx]\n" \
  "ds_read_b128 v[188:191], %[lr0] offset:35840\nds_read_b128 v[176:179], %[lr0] offset:32768\nv_exp_f32 v226, v226\nv_exp_f32 v227, v227\n" \
  "v_exp_f32 v228, v228\nv_mfma_f32_32x32x16_bf16 v[208:223], v[192:195], v[172:175], 0\nds_read_b128 v[180:183], %[lr0] offset:33792\nds_read_b128 v[184:187], %[lr0] offset:34816\n" \
  "v_exp_f32 v229, v229\nv_exp_f32 v230, v230\nv_exp_f32 v231, v231\nv_mfma_f32_32x32x16_bf16 v[208:223], v[196:199], v[168:171], v[208:223]\n" \
  "v_exp_f32 v232, v232\nv_exp_f32 v233, v233\nv_exp_f32 v234, v234\nv_exp_f32 v235, v235\n" \
  "v_smfmac_f32_32x32x32_bf16 v[240:255], v[200:203], v[224:231], %[idx]\ns_nop 1\nv_exp_f32 v236, v236\nv_exp_f32 v237, v237\n" \
  "v_exp_f32 v238, v238\nv_exp_f32 v239, v239\ns_setprio 2\nv_exp_f32 v208, v208\n" \
  "v_exp_f32 v209, v209\ns_waitcnt lgkmcnt(0)\nv_smfmac_f32_32x32x32_bf16 v[240:255], v[188:191], v[232:239], %[idx]\nds_read_b128 v[204:207], %[lr0] offset:39936\n" \
  "ds_read_b128 v[192:195], %[lr0] offset:36864\nv_exp_f32 v210, v210\nv_exp_f32 v211, v211\nv_exp_f32 v212, v212\n" \
  "v_mfma_f32_32x32x16_bf16 v[224:239], v[176:179], v[172:175], 0\nds_read_b128 v[196:199], %[lr0] offset:37888\nds_read_b128 v[200:203], %[lr0] offset:38912\nv_exp_f32 v213, v213\n" \
  "v_exp_f32 v214, v214\nv_exp_f32 v215, v215\nv_mfma_f32_32x32x16_bf16 v[224:239], v[180:183], v[168:171], v[224:239]\nv_exp_f32 v216, v216\n" \
  "v_exp_f32 v217, v217\nv_exp_f32 v218, v218\nv_exp_f32 v219, v219\nv_smfmac_f32_32x32x32_bf16 v[240:255], v[184:187], v[208:215], %[idx]\n" \
  "s_nop 1\nv_exp_f32 v220, v220\nv_exp_f32 v221, v221\nv_exp_f32 v222, v222\n" \
  "v_exp_f32 v223, v223\ns_setprio 1\nv_exp_f32 v224, v224\nv_exp_f32 v225, v225\n" \
  "s_waitcnt lgkmcnt(0)\nv_smfmac_f32_32x32x32_bf16 v[240:255], v[204:207], v[216:223], %[idx]\nds_read_b128 v[188:191], %[lr0] offset:44032\nv_exp_f32 v226, v226\n" \
  "v_exp_f32 v227, v227\nv_exp_f32 v228, v228\nv_mfma_f32_32x32x16_bf16 v[208:223], v[192:195], v[172:175], 0\nds_read_b128 v[184:187], %[lr0] offset:43008\n" \
  "v_exp_f32 v229, v229\nv_exp_f32 v230, v230\nv_exp_f32 v231, v231\nv_mfma_f32_32x32x16_bf16 v[208:223], v[196:199], v[168:171], v[208:223]\n" \
  "v_exp_f32 v232, v232\nv_exp_f32 v233, v233\nv_exp_f32 v234, v234\nv_exp_f32 v235, v235\n" \
  "v_smfmac_f32_32x32x32_bf16 v[240:255], v[200:203], v[224:231], %[idx]\ns_nop 1\nv_exp_f32 v236, v236\nv_exp_f32 v237, v237\n" \
  "v_exp_f32 v238, v238\nv_exp_f32 v239, v239\ns_setprio 0\nv_exp_f32 v208, v208\n" \
  "v_exp_f32 v209, v209\ns_waitcnt lgkmcnt(0)\nv_smfmac_f32_32x32x32_bf16 v[240:255], v[188:191], v[232:239], %[idx]\nds_read_b128 v[204:207], %[lr0] offset:48128\n" \
  "v_exp_f32 v210, v210\nv_exp_f32 v211, v211\nv_exp_f32 v212, v212\nv_exp_f32 v213, v213\n" \
  "v_exp_f32 v214, v214\nv_exp_f32 v215, v215\nv_exp_f32 v216, v216\nv_exp_f32 v217, v217\n" \
  "v_exp_f32 v218, v218\nv_exp_f32 v219, v219\nv_smfmac_f32_32x32x32_bf16 v[240:255], v[184:187], v[208:215], %[idx]\nv_exp_f32 v220, v220\n" \
  "v_exp_f32 v221, v221\nv_exp_f32 v222, v222\nv_exp_f32 v223, v223\ns_setprio 0\n" \
  "s_waitcnt vmcnt(0) lgkmcnt(0)\ns_mov_b32 m0, %[keep]\nv_smfmac_f32_32x32x32_bf16 v[240:255], v[204:207], v[216:223], %[idx]\ns_nop 15\n" \
  ""
#define ATT_ASM_H \
  "s_mov_b32 %[keep], m0\nv_mov_b32 v240, 0\nv_mov_b32 v241, 0\nv_mov_b32 v242, 0\n" \
  "v_mov_b32 v243, 0\nv_mov_b32 v244, 0\nv_mov_b32 v245, 0\nv_mov_b32 v246, 0\n" \
  "v_mov_b32 v247, 0\nv_mov_b32 v248, 0\nv_mov_b32 v249, 0\nv_mov_b32 v250, 0\n" \
  "v_mov_b32 v251, 0\nv_mov_b32 v252, 0\nv_mov_b32 v253, 0\nv_mov_b32 v254, 0\n" \
  "v_mov_b32 v255, 0\ns_waitcnt vmcnt(0) lgkmcnt(0)\ns_barrier\nds_read_b128 v[176:179], %[lr2]\n" \
  "ds_read_b128 v[180:183], %[lr2] offset:1024\nds_read_b128 v[192:195], %[lr2] offset:4096\nds_read_b128 v[196:199], %[lr2] offset:5120\nds_read_b128 v[200:203], %[lr2] offset:6144\n" \
  "s_waitcnt lgkmcnt(3)\nv_mfma_f32_32x32x16_bf16 v[224:239], v[176:179], v[172:175], 0\nv_mfma_f32_32x32x16_bf16 v[224:239], v[180:183], v[168:171], v[224:239]\ns_waitcnt lgkmcnt(0)\n" \
  "s_barrier\ns_nop 9\ns_setprio 3\nv_exp_f32 v224, v224\n" \
  "v_exp_f32 v225, v225\nds_read_b128 v[188:191], %[lr0] offset:3072\nds_read_b128 v[176:179], %[lr0] offset:0\nv_exp_f32 v226, v226\n" \
  "v_exp_f32 v227, v227\nv_exp_f32 v228, v228\nv_mfma_f32_32x32x16_bf16 v[208:223], v[192:195], v[172:175], 0\nds_read_b128 v[180:183], %[lr0] offset:1024\n" \
  "ds_read_b128 v[184:187], %[lr0] offset:2048\nv_exp_f32 v229, v229\nv_exp_f32 v230, v230\nv_exp_f32 v231, v231\n" \
  "v_mfma_f32_32x32x16_bf16 v[208:223], v[196:199], v[168:171], v[208:223]\ns_add_u32 %[ko], %[ko], 0x4000\ns_and_b32 %[ko], %[ko], 0x3ffff\ns_add_u32 m0, %[m], 32768\n" \
  "s_nop 0\nbuffer_load_dwordx4 %[l16], %[rkv], %[ko] offen offset:0 sc1 lds\nbuffer_load_dwordx4 %[l16], %[rkv], %[ko] offen offset:1024 sc1 lds\nv_exp_f32 v232, v232\n" \
  "v_exp_f32 v233, v233\nv_exp_f32 v234, v234\nv_exp_f32 v235, v235\nv_smfmac_f32_32x32x32_bf16 v[240:255], v[200:203], v[224:231], %[idx]\n" \
  "v_exp_f32 v236, v236\nv_exp_f32 v237, v237\nv_exp_f32 v238, v238\nv_exp_f32 v239, v239\n" \
  "s_setprio 2\nv_exp_f32 v208, v208\nv_exp_f32 v209, v209\ns_waitcnt lgkmcnt(0)\n" \
  "v_smfmac_f32_32x32x32_bf16 v[240:255], v[188:191], v[232:239], %[idx]\nds_read_b128 v[204:207], %[lr0] offset:7168\nds_read_b128 v[192:195], %[lr0] offset:4096\nv_exp_f32 v210, v210\n" \
  "v_exp_f32 v211, v211\nv_exp_f32 v212, v212\nv_mfma_f32_32x32x16_bf16 v[224:239], v[176:179], v[172:175], 0\nds_read_b128 v[196:199], %[lr0] offset:5120\n" \
  "ds_read_b128 v[200:203], %[lr0] offset:6144\nv_exp_f32 v213, v213\nv_exp_f32 v214, v214\nv_exp_f32 v215, v215\n" \
  "v_mfma_f32_32x32x16_bf16 v[224:239], v[180:183], v[168:171], v[224:239]\nbuffer_load_dwordx4 %[l16], %[rkv], %[ko] offen offset:2048 sc1 lds\nbuffer_load_dwordx4 %[l16], %[rkv], %[ko] offen offset:3072 sc1 lds\nv_exp_f32 v216, v216\n" \
  "v_exp_f32 v217, v217\nv_exp_f32 v218, v218\nv_exp_f32 v219, v219\nv_smfmac_f32_32x32x32_bf16 v[240:255], v[184:187], v[208:215], %[idx]\n" \
  "v_exp_f32 v220, v220\nv_exp_f32 v221, v221\nv_exp_f32 v222, v222\nv_exp_f32 v223, v223\n" \
  "s_setprio 1\nv_exp_f32 v224, v224\nv_exp_f32 v225, v225\ns_waitcnt lgkmcnt(0)\n" \
  "v_smfmac_f32_32x32x32_bf16 v[240:255], v[204:207], v[216:223], %[idx]\nds_read_b128 v[188:191], %[lr0] offset:11264\nds_read_b128 v[176:179], %[lr0] offset:8192\nv_exp_f32 v226, v226\n" \
  "v_exp_f32 v227, v227\nv_exp_f32 v228, v228\nv_mfma_f32_32x32x16_bf16 v[208:223], v[192:195], v[172:175], 0\nds_read_b128 v[180:183], %[lr0] offset:9216\n" \
  "ds_read_b128 v[184:187], %[lr0] offset:10240\nv_exp_f32 v229, v229\nv_exp_f32 v230, v230\nv_exp_f32 v231, v231\n" \
  "v_mfma_f32_32x32x16_bf16 v[208:223], v[196:199], v[168:171], v[208:223]\ns_add_u32 %[ko], %[ko], 0x4000\ns_and_b32 %[ko], %[ko], 0x3ffff\ns_add_u32 m0, %[m], 65536\n" \
  "s_nop 0\nbuffer_load_dwordx4 %[l16], %[rkv], %[ko] offen offset:0 sc1 lds\nbuffer_load_dwordx4 %[l16], %[rkv], %[ko] offen offset:1024 sc1 lds\nv_exp_f32 v232, v232\n" \
  "v_exp_f32 v233, v233\nv_exp_f32 v234, v234\nv_exp_f32 v235, v235\nv_smfmac_f32_32x32x32_bf16 v[240:255], v[200:203], v[224:231], %[idx]\n" \
  "v_exp_f32 v236, v236\nv_exp_f32 v237, v237\nv_exp_f32 v238, v238\nv_exp_f32 v239, v239\n" \
  "s_setprio 0\nv_exp_f32 v208, v208\nv_exp_f32 v209, v209\ns_waitcnt lgkmcnt(0)\n" \
  "v_smfmac_f32_32x32x32_bf16 v[240:255], v[188:191], v[232:239], %[idx]\nds_read_b128 v[204:207], %[lr0] offset:15360\nds_read_b128 v[192:195], %[lr0] offset:12288\nv_exp_f32 v210, v210\n" \
  "v_exp_f32 v211, v211\nv_exp_f32 v212, v212\nv_mfma_f32_32x32x16_bf16 v[224:239], v[176:179], v[172:175], 0\nds_read_b128 v[196:199], %[lr0] offset:13312\n" \
  "ds_read_b128 v[200:203], %[lr0] offset:14336\nv_exp_f32 v213, v213\nv_exp_f32 v214, v214\nv_exp_f32 v215, v215\n" \
  "v_mfma_f32_32x32x16_bf16 v[224:239], v[180:183], v[168:171], v[224:239]\nbuffer_load_dwordx4 %[l16], %[rkv], %[ko] offen offset:2048 sc1 lds\nbuffer_load_dwordx4 %[l16], %[rkv], %[ko] offen offset:3072 sc1 lds\nv_exp_f32 v216, v216\n" \
  "v_exp_f32 v217, v217\nv_exp_f32 v218, v218\nv_exp_f32 v219, v219\nv_smfmac_f32_32x32x32_bf16 v[240:255], v[184:187], v[208:215], %[idx]\n" \
  "v_exp_f32 v220, v220\nv_exp_f32 v221, v221\nv_exp_f32 v222, v222\nv_exp_f32 v223, v223\n" \
  "s_waitcnt vmcnt(4) lgkmcnt(0)\ns_barrier\ns_setprio 3\nv_exp_f32 v224, v224\n" \
  "v_exp_f32 v225, v225\nv_smfmac_f32_32x32x32_bf16 v[240:255], v[204:207], v[216:223], %[idx]\nds_read_b128 v[188:191], %[lr0] offset:35840\nds_read_b128 v[176:179], %[lr0] offset:32768\n" \
  "v_exp_f32 v226, v226\nv_exp_f32 v227, v227\nv_exp_f32 v228, v228\nv_mfma_f32_32x32x16_bf16 v[208:223], v[192:195], v[172:175], 0\n" \
  "ds_read_b128 v[180:183], %[lr0] offset:33792\nds_read_b128 v[184:187], %[lr0] offset:34816\nv_exp_f32 v229, v229\nv_exp_f32 v230, v230\n" \
  "v_exp_f32 v231, v231\nv_mfma_f32_32x32x16_bf16 v[208:223], v[196:199], v[168:171], v[208:223]\ns_add_u32 %[ko], %[ko], 0x4000\ns_and_b32 %[ko], %[ko], 0x3ffff\n" \
  "s_add_u32 m0, %[m], 0\ns_nop 0\nbuffer_load_dwordx4 %[l16], %[rkv], %[ko] offen offset:0 sc1 lds\nv_exp_f32 v232, v232\n" \
  "v_exp_f32 v233, v233\nv_exp_f32 v234, v234\nv_exp_f32 v235, v235\nv_smfmac_f32_32x32x32_bf16 v[240:255], v[200:203], v[224:231], %[idx]\n" \
  "v_exp_f32 v236, v236\nv_exp_f32 v237, v237\nv_exp_f32 v238, v238\nv_exp_f32 v239, v239\n" \
  "s_setprio 2\nv_exp_f32 v208, v208\nv_exp_f32 v209, v209\ns_waitcnt lgkmcnt(0)\n" \
  "v_smfmac_f32_32x32x32_bf16 v[240:255], v[188:191], v[232:239], %[idx]\nds_read_b128 v[204:207], %[lr0] offset:39936\nds_read_b128 v[192:195], %[lr0] offset:36864\nv_exp_f32 v210, v210\n" \
  "v_exp_f32 v211, v211\nv_exp_f32 v212, v212\nv_mfma_f32_32x32x16_bf16 v[224:239], v[176:179], v[172:175], 0\nds_read_b128 v[196:199], %[lr0] offset:37888\n" \
  "ds_read_b128 v[200:203], %[lr0] offset:38912\nv_exp_f32 v213, v213\nv_exp_f32 v214, v214\nv_exp_f32 v215, v215\n" \
  "v_mfma_f32_32x32x16_bf16 v[224:239], v[180:183], v[168:171], v[224:239]\nbuffer_load_dwordx4 %[l16], %[rkv], %[ko] offen offset:1024 sc1 lds\nv_exp_f32 v216, v216\nv_exp_f32 v217, v217\n" \
  "v_exp_f32 v218, v218\nv_exp_f32 v219, v219\nv_smfmac_f32_32x32x32_bf16 v[240:255], v[184:187], v[208:215], %[idx]\ns_nop 0\n" \
  "v_exp_f32 v220, v220\nv_exp_f32 v221, v221\nv_exp_f32 v222, v222\nv_exp_f32 v223, v223\n" \
  "s_setprio 1\nv_exp_f32 v224, v224\nv_exp_f32 v225, v225\ns_waitcnt lgkmcnt(0)\n" \
  "v_smfmac_f32_32x32x32_bf16 v[240:255], v[204:207], v[216:223], %[idx]\nds_read_b128 v[188:191], %[lr0] offset:44032\nds_read_b128 v[176:179], %[lr0] offset:40960\nv_exp_f32 v226, v226\n" \
  "v_exp_f32 v227, v227\nv_exp_f32 v228, v228\nv_mfma_f32_32x32x16_bf16 v[208:223], v[192:195], v[172:175], 0\nds_read_b128 v[180:183], %[lr0] offset:41984\n" \
  "ds_read_b128 v[184:187], %[lr0] offset:43008\nv_exp_f32 v229, v229\nv_exp_f32 v230, v230\nv_exp_f32 v231, v231\n" \
  "v_mfma_f32_32x32x16_bf16 v[208:223], v[196:199], v[168:171], v[208:223]\nbuffer_load_dwordx4 %[l16], %[rkv], %[ko] offen offset:2048 sc1 lds\nv_exp_f32 v232, v232\nv_exp_f32 v233, v233\n" \
  "v_exp_f32 v234, v234\nv_exp_f32 v235, v235\nv_smfmac_f32_32x32x32_bf16 v[240:255], v[200:203], v[224:231], %[idx]\ns_nop 0\n" \
  "v_exp_f32 v236, v236\nv_exp_f32 v237, v237\nv_exp_f32 v238, v238\nv_exp_f32 v239, v239\n" \
  "s_setprio 0\nv_exp_f32 v208, v208\nv_exp_f32 v209, v209\ns_waitcnt lgkmcnt(0)\n" \
  "v_smfmac_f32_32x32x32_bf16 v[240:255], v[188:191], v[232:239], %[idx]\nds_read_b128 v[204:207], %[lr0] offset:48128\nds_read_b128 v[192:195], %[lr0] offset:45056\nv_exp_f32 v210, v210\n" \
  "v_exp_f32 v211, v211\nv_exp_f32 v212, v212\nv_mfma_f32_32x32x16_bf16 v[224:239], v[176:179], v[172:175], 0\nds_read_b128 v[196:199], %[lr0] offset:46080\n" \
  "ds_read_b128 v[200:203], %[lr0] offset:47104\nv_exp_f32 v213, v213\nv_exp_f32 v214, v214\nv_exp_f32 v215, v215\n" \
  "v_mfma_f32_32x32x16_bf16 v[224:239], v[180:183], v[168:171], v[224:239]\nbuffer_load_dwordx4 %[l16], %[rkv], %[ko] offen offset:3072 sc1 lds\nv_exp_f32 v216, v216\nv_exp_f32 v217, v217\n" \
  "v_exp_f32 v218, v218\nv_exp_f32 v219, v219\nv_smfmac_f32_32x32x32_bf16 v[240:255], v[184:187], v[208:215], %[idx]\ns_nop 0\n" \
  "v_exp_f32 v220, v220\nv_exp_f32 v221, v221\nv_exp_f32 v222, v222\nv_exp_f32 v223, v223\n" \
  "s_waitcnt vmcnt(4) lgkmcnt(0)\ns_barrier\ns_setprio 3\nv_exp_f32 v224, v224\n" \
  "v_exp_f32 v225, v225\nv_smfmac_f32_32x32x32_bf16 v[240:255], v[204:207], v[216:223], %[idx]\nds_read_b128 v[188:191], %[lr2] offset:3072\nds_read_b128 v[176:179], %[lr2] offset:0\n" \
  "v_exp_f32 v226, v226\nv_exp_f32 v227, v227\nv_exp_f32 v228, v228\nv_mfma_f32_32x32x16_bf16 v[208:223], v[192:195], v[172:175], 0\n" \
  "ds_read_b128 v[180:183], %[lr2] offset:1024\nds_read_b128 v[184:187], %[lr2] offset:2048\nv_exp_f32 v229, v229\nv_exp_f32 v230, v230\n" \
  "v_exp_f32 v231, v231\nv_mfma_f32_32x32x16_bf16 v[208:223], v[196:199], v[168:171], v[208:223]\ns_add_u32 %[ko], %[ko], 0x4000\ns_and_b32 %[ko], %[ko], 0x3ffff\n" \
  "s_add_u32 m0, %[m], 32768\ns_nop 0\nbuffer_load_dwordx4 %[l16], %[rkv], %[ko] offen offset:0 sc1 lds\nv_exp_f32 v232, v232\n" \
  "v_exp_f32 v233, v233\nv_exp_f32 v234, v234\nv_exp_f32 v235, v235\nv_smfmac_f32_32x32x32_bf16 v[240:255], v[200:203], v[224:231], %[idx]\n" \
  "v_exp_f32 v236, v236\nv_exp_f32 v237, v237\nv_exp_f32 v238, v238\nv_exp_f32 v239, v239\n" \
  "s_setprio 2\nv_exp_f32 v208, v208\nv_exp_f32 v209, v209\ns_waitcnt lgkmcnt(0)\n" \
  "v_smfmac_f32_32x32x32_bf16 v[240:255], v[188:191], v[232:239], %[idx]\nds_read_b128 v[204:207], %[lr2] offset:7168\nds_read_b128 v[192:195], %[lr2] offset:4096\nv_exp_f32 v210, v210\n" \
  "v_exp_f32 v211, v211\nv_exp_f32 v212, v212\nv_mfma_f32_32x32x16_bf16 v[224:239], v[176:179], v[172:175], 0\nds_read_b128 v[196:199], %[lr2] offset:5120\n" \
  "ds_read_b128 v[200:203], %[lr2] offset:6144\nv_exp_f32 v213, v213\nv_exp_f32 v214, v214\nv_exp_f32 v215, v215\n" \
  "v_mfma_f32_32x32x16_bf16 v[224:239], v[180:183], v[168:171], v[224:239]\nbuffer_load_dwordx4 %[l16], %[rkv], %[ko] offen offset:1024 sc1 lds\nv_exp_f32 v216, v216\nv_exp_f32 v217, v217\n" \
  "v_exp_f32 v218, v218\nv_exp_f32 v219, v219\nv_smfmac_f32_32x32x32_bf16 v[240:255], v[184:187], v[208:215], %[idx]\ns_nop 0\n" \
  "v_exp_f32 v220, v220\nv_exp_f32 v221, v221\nv_exp_f32 v222, v222\nv_exp_f32 v223, v223\n" \
  "s_setprio 1\nv_exp_f32 v224, v224\nv_exp_f32 v225, v225\ns_waitcnt lgkmcnt(0)\n" \
  "v_smfmac_f32_32x32x32_bf16 v[240:255], v[204:207], v[216:223], %[idx]\nds_read_b128 v[188:191], %[lr2] offset:11264\nds_read_b128 v[176:179], %[lr2] offset:8192\nv_exp_f32 v226, v226\n" \
  "v_exp_f32 v227, v227\nv_exp_f32 v228, v228\nv_mfma_f32_32x32x16_bf16 v[208:223], v[192:195], v[172:175], 0\nds_read_b128 v[180:183], %[lr2] offset:9216\n" \
  "ds_read_b128 v[184:187], %[lr2] offset:10240\nv_exp_f32 v229, v229\nv_exp_f32 v230, v230\nv_exp_f32 v231, v231\n" \
  "v_mfma_f32_32x32x16_bf16 v[208:223], v[196:199], v[168:171], v[208:223]\nbuffer_load_dwordx4 %[l16], %[rkv], %[ko] offen offset:2048 sc1 lds\nv_exp_f32 v232, v232\nv_exp_f32 v233, v233\n" \
  "v_exp_f32 v234, v234\nv_exp_f32 v235, v235\nv_smfmac_f32_32x32x32_bf16 v[240:255], v[200:203], v[224:231], %[idx]\ns_nop 0\n" \
  "v_exp_f32 v236, v236\nv_exp_f32 v237, v237\nv_exp_f32 v238, v238\nv_exp_f32 v239, v239\n" \
  "s_setprio 0\nv_exp_f32 v208, v208\nv_exp_f32 v209, v209\ns_waitcnt lgkmcnt(0)\n" \
  "v_smfmac_f32_32x32x32_bf16 v[240:255], v[188:191], v[232:239], %[idx]\nds_read_b128 v[204:207], %[lr2] offset:15360\nds_read_b128 v[192:195], %[lr2] offset:12288\nv_exp_f32 v210, v210\n" \
  "v_exp_f32 v211, v211\nv_exp_f32 v212, v212\nv_mfma_f32_32x32x16_bf16 v[224:239], v[176:179], v[172:175], 0\nds_read_b128 v[196:199], %[lr2] offset:13312\n" \
  "ds_read_b128 v[200:203], %[lr2] offset:14336\nv_exp_f32 v213, v213\nv_exp_f32 v214, v214\nv_exp_f32 v215, v215\n" \
  "v_mfma_f32_32x32x16_bf16 v[224:239], v[180:183], v[168:171], v[224:239]\nbuffer_load_dwordx4 %[l16], %[rkv], %[ko] offen offset:3072 sc1 lds\nv_exp_f32 v216, v216\nv_exp_f32 v217, v217\n" \
  "v_exp_f32 v218, v218\nv_exp_f32 v219, v219\nv_smfmac_f32_32x32x32_bf16 v[240:255], v[184:187], v[208:215], %[idx]\ns_nop 0\n" \
  "v_exp_f32 v220, v220\nv_exp_f32 v221, v221\nv_exp_f32 v222, v222\nv_exp_f32 v223, v223\n" \
  "s_waitcnt vmcnt(4) lgkmcnt(0)\ns_barrier\ns_setprio 3\nv_exp_f32 v224, v224\n" \
  "v_exp_f32 v225, v225\nv_smfmac_f32_32x32x32_bf16 v[240:255], v[204:207], v[216:223], %[idx]\nds_read_b128 v[188:191], %[lr0] offset:3072\nds_read_b128 v[176:179], %[lr0] offset:0\n" \
  "v_exp_f32 v226, v226\nv_exp_f32 v227, v227\nv_exp_f32 v228, v228\nv_mfma_f32_32x32x16_bf16 v[208:223], v[192:195], v[172:175], 0\n" \
  "ds_read_b128 v[180:183], %[lr0] offset:1024\nds_read_b128 v[184:187], %[lr0] offset:2048\nv_exp_f32 v229, v229\nv_exp_f32 v230, v230\n" \
  "v_exp_f32 v231, v231\nv_mfma_f32_32x32x16_bf16 v[208:223], v[196:199], v[168:171], v[208:223]\ns_add_u32 %[ko], %[ko], 0x4000\ns_and_b32 %[ko], %[ko], 0x3ffff\n" \
  "s_add_u32 m0, %[m], 65536\ns_nop 0\nbuffer_load_dwordx4 %[l16], %[rkv], %[ko] offen offset:0 sc1 lds\nv_exp_f32 v232, v232\n" \
  "v_exp_f32 v233, v233\nv_exp_f32 v234, v234\nv_exp_f32 v235, v235\nv_smfmac_f32_32x32x32_bf16 v[240:255], v[200:203], v[224:231], %[idx]\n" \
  "v_exp_f32 v236, v236\nv_exp_f32 v237, v237\nv_exp_f32 v238, v238\nv_exp_f32 v239, v239\n" \
  "s_setprio 2\nv_exp_f32 v208, v208\nv_exp_f32 v209, v209\ns_waitcnt lgkmcnt(0)\n" \
  "v_smfmac_f32_32x32x32_bf16 v[240:255], v[188:191], v[232:239], %[idx]\nds_read_b128 v[204:207], %[lr0] offset:7168\nds_read_b128 v[192:195], %[lr0] offset:4096\nv_exp_f32 v210, v210\n" \
  "v_exp_f32 v211, v211\nv_exp_f32 v212, v212\nv_mfma_f32_32x32x16_bf16 v[224:239], v[176:179], v[172:175], 0\nds_read_b128 v[196:199], %[lr0] offset:5120\n" \
  "ds_read_b128 v[200:203], %[lr0] offset:6144\nv_exp_f32 v213, v213\nv_exp_f32 v214, v214\nv_exp_f32 v215, v215\n" \
  "v_mfma_f32_32x32x16_bf16 v[224:239], v[180:183], v[168:171], v[224:239]\nbuffer_load_dwordx4 %[l16], %[rkv], %[ko] offen offset:1024 sc1 lds\nv_exp_f32 v216, v216\nv_exp_f32 v217, v217\n" \
  "v_exp_f32 v218, v218\nv_exp_f32 v219, v219\nv_smfmac_f32_32x32x32_bf16 v[240:255], v[184:187], v[208:215], %[idx]\ns_nop 0\n" \
  "v_exp_f32 v220, v220\nv_exp_f32 v221, v221\nv_exp_f32 v222, v222\nv_exp_f32 v223, v223\n" \
  "s_setprio 1\nv_exp_f32 v224, v224\nv_exp_f32 v225, v225\ns_waitcnt lgkmcnt(0)\n" \
  "v_smfmac_f32_32x32x32_bf16 v[240:255], v[204:207], v[216:223], %[idx]\nds_read_b128 v[188:191], %[lr0] offset:11264\nds_read_b128 v[176:179], %[lr0] offset:8192\nv_exp_f32 v226, v226\n" \
  "v_exp_f32 v227, v227\nv_exp_f32 v228, v228\nv_mfma_f32_32x32x16_bf16 v[208:223], v[192:195], v[172:175], 0\nds_read_b128 v[180:183], %[lr0] offset:9216\n" \
  "ds_read_b128 v[184:187], %[lr0] offset:10240\nv_exp_f32 v229, v229\nv_exp_f32 v230, v230\nv_exp_f32 v231, v231\n" \
  "v_mfma_f32_32x32x16_bf16 v[208:223], v[196:199], v[168:171], v[208:223]\nbuffer_load_dwordx4 %[l16], %[rkv], %[ko] offen offset:2048 sc1 lds\nv_exp_f32 v232, v232\nv_exp_f32 v233, v233\n" \
  "v_exp_f32 v234, v234\nv_exp_f32 v235, v235\nv_smfmac_f32_32x32x32_bf16 v[240:255], v[200:203], v[224:231], %[idx]\ns_nop 0\n" \
  "v_exp_f32 v236, v236\nv_exp_f32 v237, v237\nv_exp_f32 v238, v238\nv_exp_f32 v239, v239\n" \
  "s_setprio 0\nv_exp_f32 v208, v208\nv_exp_f32 v209, v209\ns_waitcnt lgkmcnt(0)\n" \
  "v_smfmac_f32_32x32x32_bf16 v[240:255], v[188:191], v[232:239], %[idx]\nds_read_b128 v[204:207], %[lr0] offset:15360\nds_read_b128 v[192:195], %[lr0] offset:12288\nv_exp_f32 v210, v210\n" \
  "v_exp_f32 v211, v211\nv_exp_f32 v212, v212\nv_mfma_f32_32x32x16_bf16 v[224:239], v[176:179], v[172:175], 0\nds_read_b128 v[196:199], %[lr0] offset:13312\n" \
  "ds_read_b128 v[200:203], %[lr0] offset:14336\nv_exp_f32 v213, v213\nv_exp_f32 v214, v214\nv_exp_f32 v215, v215\n" \
  "v_mfma_f32_32x32x16_bf16 v[224:239], v[180:183], v[168:171], v[224:239]\nbuffer_load_dwordx4 %[l16], %[rkv], %[ko] offen offset:3072 sc1 lds\nv_exp_f32 v216, v216\nv_exp_f32 v217, v217\n" \
  "v_exp_f32 v218, v218\nv_exp_f32 v219, v219\nv_smfmac_f32_32x32x32_bf16 v[240:255], v[184:187], v[208:215], %[idx]\ns_nop 0\n" \
  "v_exp_f32 v220, v220\nv_exp_f32 v221, v221\nv_exp_f32 v222, v222\nv_exp_f32 v223, v223\n" \
  "s_waitcnt vmcnt(4) lgkmcnt(0)\ns_barrier\ns_setprio 3\nv_exp_f32 v224, v224\n" \
  "v_exp_f32 v225, v225\nv_smfmac_f32_32x32x32_bf16 v[240:255], v[204:207], v[216:223], %[idx]\nds_read_b128 v[188:191], %[lr0] offset:35840\nds_read_b128 v[176:179], %[lr0] offset:32768\n" \
  "v_exp_f32 v226, v226\nv_exp_f32 v227, v227\nv_exp_f32 v228, v228\nv_mfma_f32_32x32x16_bf16 v[208:223], v[192:195], v[172:175], 0\n" \
  "ds_read_b128 v[180:183], %[lr0] offset:33792\nds_read_b128 v[184:187], %[lr0] offset:34816\nv_exp_f32 v229, v229\nv_exp_f32 v230, v230\n" \
  "v_exp_f32 v231, v231\nv_mfma_f32_32x32x16_bf16 v[208:223], v[196:199], v[168:171], v[208:223]\ns_add_u32 %[ko], %[ko], 0x4000\ns_and_b32 %[ko], %[ko], 0x3ffff\n" \
  "s_add_u32 m0, %[m], 0\ns_nop 0\nbuffer_load_dwordx4 %[l16], %[rkv], %[ko] offen offset:0 sc1 lds\nv_exp_f32 v232, v232\n" \
  "v_exp_f32 v233, v233\nv_exp_f32 v234, v234\nv_exp_f32 v235, v235\nv_smfmac_f32_32x32x32_bf16 v[240:255], v[200:203], v[224:231], %[idx]\n" \
  "v_exp_f32 v236, v236\nv_exp_f32 v237, v237\nv_exp_f32 v238, v238\nv_exp_f32 v239, v239\n" \
  "s_setprio 2\nv_exp_f32 v208, v208\nv_exp_f32 v209, v209\ns_waitcnt lgkmcnt(0)\n" \
  "v_smfmac_f32_32x32x32_bf16 v[240:255], v[188:191], v[232:239], %[idx]\nds_read_b128 v[204:207], %[lr0] offset:39936\nds_read_b128 v[192:195], %[lr0] offset:36864\nv_exp_f32 v210, v210\n" \
  "v_exp_f32 v211, v211\nv_exp_f32 v212, v212\nv_mfma_f32_32x32x16_bf16 v[224:239], v[176:179], v[172:175], 0\nds_read_b128 v[196:199], %[lr0] offset:37888\n" \
  "ds_read_b128 v[200:203], %[lr0] offset:38912\nv_exp_f32 v213, v213\nv_exp_f32 v214, v214\nv_exp_f32 v215, v215\n" \
  "v_mfma_f32_32x32x16_bf16 v[224:239], v[180:183], v[168:171], v[224:239]\nbuffer_load_dwordx4 %[l16], %[rkv], %[ko] offen offset:1024 sc1 lds\nv_exp_f32 v216, v216\nv_exp_f32 v217, v217\n" \
  "v_exp_f32 v218, v218\nv_exp_f32 v219, v219\nv_smfmac_f32_32x32x32_bf16 v[240:255], v[184:187], v[208:215], %[idx]\ns_nop 0\n" \
  "v_exp_f32 v220, v220\nv_exp_f32 v221, v221\nv_exp_f32 v222, v222\nv_exp_f32 v223, v223\n" \
  "s_setprio 1\nv_exp_f32 v224, v224\nv_exp_f32 v225, v225\ns_waitcnt lgkmcnt(0)\n" \
  "v_smfmac_f32_32x32x32_bf16 v[240:255], v[204:207], v[216:223], %[idx]\nds_read_b128 v[188:191], %[lr0] offset:44032\nds_read_b128 v[176:179], %[lr0] offset:40960\nv_exp_f32 v226, v226\n" \
  "v_exp_f32 v227, v227\nv_exp_f32 v228, v228\nv_mfma_f32_32x32x16_bf16 v[208:223], v[192:195], v[172:175], 0\nds_read_b128 v[180:183], %[lr0] offset:41984\n" \
  "ds_read_b128 v[184:187], %[lr0] offset:43008\nv_exp_f32 v229, v229\nv_exp_f32 v230, v230\nv_exp_f32 v231, v231\n" \
  "v_mfma_f32_32x32x16_bf16 v[208:223], v[196:199], v[168:171], v[208:223]\nbuffer_load_dwordx4 %[l16], %[rkv], %[ko] offen offset:2048 sc1 lds\nv_exp_f32 v232, v232\nv_exp_f32 v233, v233\n" \
  "v_exp_f32 v234, v234\nv_exp_f32 v235, v235\nv_smfmac_f32_32x32x32_bf16 v[240:255], v[200:203], v[224:231], %[idx]\ns_nop 0\n" \
  "v_exp_f32 v236, v236\nv_exp_f32 v237, v237\nv_exp_f32 v238, v238\nv_exp_f32 v239, v239\n" \
  "s_setprio 0\nv_exp_f32 v208, v208\nv_exp_f32 v209, v209\ns_waitcnt lgkmcnt(0)\n" \
  "v_smfmac_f32_32x32x32_bf16 v[240:255], v[188:191], v[232:239], %[idx]\nds_read_b128 v[204:207], %[lr0] offset:48128\nds_read_b128 v[192:195], %[lr0] offset:45056\nv_exp_f32 v210, v210\n" \
  "v_exp_f32 v211, v211\nv_exp_f32 v212, v212\nv_mfma_f32_32x32x16_bf16 v[224:239], v[176:179], v[172:175], 0\nds_read_b128 v[196:199], %[lr0] offset:46080\n" \
  "ds_read_b128 v[200:203], %[lr0] offset:47104\nv_exp_f32 v213, v213\nv_exp_f32 v214, v214\nv_exp_f32 v215, v215\n" \
  "v_mfma_f32_32x32x16_bf16 v[224:239], v[180:183], v[168:171], v[224:239]\nbuffer_load_dwordx4 %[l16], %[rkv], %[ko] offen offset:3072 sc1 lds\nv_exp_f32 v216, v216\nv_exp_f32 v217, v217\n" \
  "v_exp_f32 v218, v218\nv_exp_f32 v219, v219\nv_smfmac_f32_32x32x32_bf16 v[240:255], v[184:187], v[208:215], %[idx]\ns_nop 0\n" \
  "v_exp_f32 v220, v220\nv_exp_f32 v221, v221\nv_exp_f32 v222, v222\nv_exp_f32 v223, v223\n" \
  "s_waitcnt vmcnt(4) lgkmcnt(0)\ns_barrier\ns_setprio 3\nv_exp_f32 v224, v224\n" \
  "v_exp_f32 v225, v225\nv_smfmac_f32_32x32x32_bf16 v[240:255], v[204:207], v[216:223], %[idx]\nds_read_b128 v[188:191], %[lr2] offset:3072\nds_read_b128 v[176:179], %[lr2] offset:0\n" \
  "v_exp_f32 v226, v226\nv_exp_f32 v227, v227\nv_exp_f32 v228, v228\nv_mfma_f32_32x32x16_bf16 v[208:223], v[192:195], v[172:175], 0\n" \
  "ds_read_b128 v[180:183], %[lr2] offset:1024\nds_read_b128 v[184:187], %[lr2] offset:2048\nv_exp_f32 v229, v229\nv_exp_f32 v230, v230\n" \
  "v_exp_f32 v231, v231\nv_mfma_f32_32x32x16_bf16 v[208:223], v[196:199], v[168:171], v[208:223]\ns_add_u32 %[ko], %[ko], 0x4000\ns_and_b32 %[ko], %[ko], 0x3ffff\n" \
  "s_add_u32 m0, %[m], 32768\ns_nop 0\nbuffer_load_dwordx4 %[l16], %[rkv], %[ko] offen offset:0 sc1 lds\nv_exp_f32 v232, v232\n" \
  "v_exp_f32 v233, v233\nv_exp_f32 v234, v234\nv_exp_f32 v235, v235\nv_smfmac_f32_32x32x32_bf16 v[240:255], v[200:203], v[224:231], %[idx]\n" \
  "v_exp_f32 v236, v236\nv_exp_f32 v237, v237\nv_exp_f32 v238, v238\nv_exp_f32 v239, v239\n" \
  "s_setprio 2\nv_exp_f32 v208, v208\nv_exp_f32 v209, v209\ns_waitcnt lgkmcnt(0)\n" \
  "v_smfmac_f32_32x32x32_bf16 v[240:255], v[188:191], v[232:239], %[idx]\nds_read_b128 v[204:207], %[lr2] offset:7168\nds_read_b128 v[192:195], %[lr2] offset:4096\nv_exp_f32 v210, v210\n" \
  "v_exp_f32 v211, v211\nv_exp_f32 v212, v212\nv_mfma_f32_32x32x16_bf16 v[224:239], v[176:179], v[172:175], 0\nds_read_b128 v[196:199], %[lr2] offset:5120\n" \
  "ds_read_b128 v[200:203], %[lr2] offset:6144\nv_exp_f32 v213, v213\nv_exp_f32 v214, v214\nv_exp_f32 v215, v215\n" \
  "v_mfma_f32_32x32x16_bf16 v[224:239], v[180:183], v[168:171], v[224:239]\nbuffer_load_dwordx4 %[l16], %[rkv], %[ko] offen offset:1024 sc1 lds\nv_exp_f32 v216, v216\nv_exp_f32 v217, v217\n" \
  "v_exp_f32 v218, v218\nv_exp_f32 v219, v219\nv_smfmac_f32_32x32x32_bf16 v[240:255], v[184:187], v[208:215], %[idx]\ns_nop 0\n" \
  "v_exp_f32 v220, v220\nv_exp_f32 v221, v221\nv_exp_f32 v222, v222\nv_exp_f32 v223, v223\n" \
  "s_setprio 1\nv_exp_f32 v224, v224\nv_exp_f32 v225, v225\ns_waitcnt lgkmcnt(0)\n" \
  "v_smfmac_f32_32x32x32_bf16 v[240:255], v[204:207], v[216:223], %[idx]\nds_read_b128 v[188:191], %[lr2] offset:11264\nds_read_b128 v[176:179], %[lr2] offset:8192\nv_exp_f32 v226, v226\n" \
  "v_exp_f32 v227, v227\nv_exp_f32 v228, v228\nv_mfma_f32_32x32x16_bf16 v[208:223], v[192:195], v[172:175], 0\nds_read_b128 v[180:183], %[lr2] offset:9216\n" \
  "ds_read_b128 v[184:187], %[lr2] offset:10240\nv_exp_f32 v229, v229\nv_exp_f32 v230, v230\nv_exp_f32 v231, v231\n" \
  "v_mfma_f32_32x32x16_bf16 v[208:223], v[196:199], v[168:171], v[208:223]\nbuffer_load_dwordx4 %[l16], %[rkv], %[ko] offen offset:2048 sc1 lds\nv_exp_f32 v232, v232\nv_exp_f32 v233, v233\n" \
  "v_exp_f32 v234, v234\nv_exp_f32 v235, v235\nv_smfmac_f32_32x32x32_bf16 v[240:255], v[200:203], v[224:231], %[idx]\ns_nop 0\n" \
  "v_exp_f32 v236, v236\nv_exp_f32 v237, v237\nv_exp_f32 v238, v238\nv_exp_f32 v239, v239\n" \
  "s_setprio 0\nv_exp_f32 v208, v208\nv_exp_f32 v209, v209\ns_waitcnt lgkmcnt(0)\n" \
  "v_smfmac_f32_32x32x32_bf16 v[240:255], v[188:191], v[232:239], %[idx]\nds_read_b128 v[204:207], %[lr2] offset:15360\nds_read_b128 v[192:195], %[lr2] offset:12288\nv_exp_f32 v210, v210\n" \
  "v_exp_f32 v211, v211\nv_exp_f32 v212, v212\nv_mfma_f32_32x32x16_bf16 v[224:239], v[176:179], v[172:175], 0\nds_read_b128 v[196:199], %[lr2] offset:13312\n" \
  "ds_read_b128 v[200:203], %[lr2] offset:14336\nv_exp_f32 v213, v213\nv_exp_f32 v214, v214\nv_exp_f32 v215, v215\n" \
  "v_mfma_f32_32x32x16_bf16 v[224:239], v[180:183], v[168:171], v[224:239]\nbuffer_load_dwordx4 %[l16], %[rkv], %[ko] offen offset:3072 sc1 lds\nv_exp_f32 v216, v216\nv_exp_f32 v217, v217\n" \
  "v_exp_f32 v218, v218\nv_exp_f32 v219, v219\nv_smfmac_f32_32x32x32_bf16 v[240:255], v[184:187], v[208:215], %[idx]\ns_nop 0\n" \
  "v_exp_f32 v220, v220\nv_exp_f32 v221, v221\nv_exp_f32 v222, v222\nv_exp_f32 v223, v223\n" \
  "s_waitcnt vmcnt(4) lgkmcnt(0)\ns_barrier\ns_setprio 3\nv_exp_f32 v224, v224\n" \
  "v_exp_f32 v225, v225\nv_smfmac_f32_32x32x32_bf16 v[240:255], v[204:207], v[216:223], %[idx]\nds_read_b128 v[188:191], %[lr0] offset:3072\nds_read_b128 v[176:179], %[lr0] offset:0\n" \
  "v_exp_f32 v226, v226\nv_exp_f32 v227, v227\nv_exp_f32 v228, v228\nv_mfma_f32_32x32x16_bf16 v[208:223], v[192:195], v[172:175], 0\n" \
  "ds_read_b128 v[180:183], %[lr0] offset:1024\nds_read_b128 v[184:187], %[lr0] offset:2048\nv_exp_f32 v229, v229\nv_exp_f32 v230, v230\n" \
  "v_exp_f32 v231, v231\nv_mfma_f32_32x32x16_bf16 v[208:223], v[196:199], v[168:171], v[208:223]\nv_exp_f32 v232, v232\nv_exp_f32 v233, v233\n" \
  "v_exp_f32 v234, v234\nv_exp_f32 v235, v235\nv_smfmac_f32_32x32x32_bf16 v[240:255], v[200:203], v[224:231], %[idx]\ns_nop 1\n" \
  "v_exp_f32 v236, v236\nv_exp_f32 v237, v237\nv_exp_f32 v238, v238\nv_exp_f32 v239, v239\n" \
  "s_setprio 2\nv_exp_f32 v208, v208\nv_exp_f32 v209, v209\ns_waitcnt lgkmcnt(0)\n" \
  "v_smfmac_f32_32x32x32_bf16 v[240:255], v[188:191], v[232:239], %[idx]\nds_read_b128 v[204:207], %[lr0] offset:7168\nds_read_b128 v[192:195], %[lr0] offset:4096\nv_exp_f32 v210, v210\n" \
  "v_exp_f32 v211, v211\nv_exp_f32 v212, v212\nv_mfma_f32_32x32x16_bf16 v[224:239], v[176:179], v[172:175], 0\nds_read_b128 v[196:199], %[lr0] offset:5120\n" \
  "ds_read_b128 v[200:203], %[lr0] offset:6144\nv_exp_f32 v213, v213\nv_exp_f32 v214, v214\nv_exp_f32 v215, v215\n" \
  "v_mfma_f32_32x32x16_bf16 v[224:239], v[180:183], v[168:171], v[224:239]\nv_exp_f32 v216, v216\nv_exp_f32 v217, v217\nv_exp_f32 v218, v218\n" \
  "v_exp_f32 v219, v219\nv_smfmac_f32_32x32x32_bf16 v[240:255], v[184:187], v[208:215], %[idx]\ns_nop 1\nv_exp_f32 v220, v220\n" \
  "v_exp_f32 v221, v221\nv_exp_f32 v222, v222\nv_exp_f32 v223, v223\ns_setprio 1\n" \
  "v_exp_f32 v224, v224\nv_exp_f32 v225, v225\ns_waitcnt lgkmcnt(0)\nv_smfmac_f32_32x32x32_bf16 v[240:255], v[204:207], v[216:223], %[idx]\n" \
  "ds_read_b128 v[188:191], %[lr0] offset:11264\nds_read_b128 v[176:179], %[lr0] offset:8192\nv_exp_f32 v226, v226\nv_exp_f32 v227, v227\n" \
  "v_exp_f32 v228, v228\nv_mfma_f32_32x32x16_bf16 v[208:223], v[192:195], v[172:175], 0\nds_read_b128 v[180:183], %[lr0] offset:9216\nds_read_b128 v[184:187], %[lr0] offset:10240\n" \
  "v_exp_f32 v229, v229\nv_exp_f32 v230, v230\nv_exp_f32 v231, v231\nv_mfma_f32_32x32x16_bf16 v[208:223], v[196:199], v[168:171], v[208:223]\n" \
  "v_exp_f32 v232, v232\nv_exp_f32 v233, v233\nv_exp_f32 v234, v234\nv_exp_f32 v235, v235\n" \
  "v_smfmac_f32_32x32x32_bf16 v[240:255], v[200:203], v[224:231], %[idx]\ns_nop 1\nv_exp_f32 v236, v236\nv_exp_f32 v237, v237\n" \
  "v_exp_f32 v238, v238\nv_exp_f32 v239, v239\ns_setprio 0\nv_exp_f32 v208, v208\n" \
  "v_exp_f32 v209, v209\ns_waitcnt lgkmcnt(0)\nv_smfmac_f32_32x32x32_bf16 v[240:255], v[188:191], v[232:239], %[idx]\nds_read_b128 v[204:207], %[lr0] offset:15360\n" \
  "ds_read_b128 v[192:195], %[lr0] offset:12288\nv_exp_f32 v210, v210\nv_exp_f32 v211, v211\nv_exp_f32 v212, v212\n" \
  "v_mfma_f32_32x32x16_bf16 v[224:239], v[176:179], v[172:175], 0\nds_read_b128 v[196:199], %[lr0] offset:13312\nds_read_b128 v[200:203], %[lr0] offset:14336\nv_exp_f32 v213, v213\n" \
  "v_exp_f32 v214, v214\nv_exp_f32 v215, v215\nv_mfma_f32_32x32x16_bf16 v[224:239], v[180:183], v[168:171], v[224:239]\nv_exp_f32 v216, v216\n" \
  "v_exp_f32 v217, v217\nv_exp_f32 v218, v218\nv_exp_f32 v219, v219\nv_smfmac_f32_32x32x32_bf16 v[240:255], v[184:187], v[208:215], %[idx]\n" \
  "s_nop 1\nv_exp_f32 v220, v220\nv_exp_f32 v221, v221\nv_exp_f32 v222, v222\n" \
  "v_exp_f32 v223, v223\ns_waitcnt vmcnt(0) lgkmcnt(0)\ns_barrier\ns_setprio 3\n" \
  "v_exp_f32 v224, v224\nv_exp_f32 v225, v225\nv_smfmac_f32_32x32x32_bf16 v[240:255], v[204:207], v[216:223], %[idx]\nds_read_b128 v[188:191], %[lr0] offset:35840\n" \
  "ds_read_b128 v[176:179], %[lr0] offset:32768\nv_exp_f32 v226, v226\nv_exp_f32 v227, v227\nv_exp_f32 v228, v228\n" \
  "v_mfma_f32_32x32x16_bf16 v[208:223], v[192:195], v[172:175], 0\nds_read_b128 v[180:183], %[lr0] offset:33792\nds_read_b128 v[184:187], %[lr0] offset:34816\nv_exp_f32 v229, v229\n" \
  "v_exp_f32 v230, v230\nv_exp_f32 v231, v231\nv_mfma_f32_32x32x16_bf16 v[208:223], v[196:199], v[168:171], v[208:223]\nv_exp_f32 v232, v232\n" \
  "v_exp_f32 v233, v233\nv_exp_f32 v234, v234\nv_exp_f32 v235, v235\nv_smfmac_f32_32x32x32_bf16 v[240:255], v[200:203], v[224:231], %[idx]\n" \
  "s_nop 1\nv_exp_f32 v236, v236\nv_exp_f32 v237, v237\nv_exp_f32 v238, v238\n" \
  "v_exp_f32 v239, v239\ns_setprio 2\nv_exp_f32 v208, v208\nv_exp_f32 v209, v209\n" \
  "s_waitcnt lgkmcnt(0)\nv_smfmac_f32_32x32x32_bf16 v[240:255], v[188:191], v[232:239], %[idx]\nds_read_b128 v[204:207], %[lr0] offset:39936\nds_read_b128 v[192:195], %[lr0] offset:36864\n" \
  "v_exp_f32 v210, v210\nv_exp_f32 v211, v211\nv_exp_f32 v212, v212\nv_mfma_f32_32x32x16_bf16 v[224:239], v[176:179], v[172:175], 0\n" \
  "ds_read_b128 v[196:199], %[lr0] offset:37888\nds_read_b128 v[200:203], %[lr0] offset:38912\nv_exp_f32 v213, v213\nv_exp_f32 v214, v214\n" \
  "v_exp_f32 v215, v215\nv_mfma_f32_32x32x16_bf16 v[224:239], v[180:183], v[168:171], v[224:239]\nv_exp_f32 v216, v216\nv_exp_f32 v217, v217\n" \
  "v_exp_f32 v218, v218\nv_exp_f32 v219, v219\nv_smfmac_f32_32x32x32_bf16 v[240:255], v[184:187], v[208:215], %[idx]\ns_nop 1\n" \
  "v_exp_f32 v220, v220\nv_exp_f32 v221, v221\nv_exp_f32 v222, v222\nv_exp_f32 v223, v223\n" \
  "s_setprio 1\nv_exp_f32 v224, v224\nv_exp_f32 v225, v225\ns_waitcnt lgkmcnt(0)\n" \
  "v_smfmac_f32_32x32x32_bf16 v[240:255], v[204:207], v[216:223], %[idx]\nds_read_b128 v[188:191], %[lr0] offset:44032\nv_exp_f32 v226, v226\nv_exp_f32 v227, v227\n" \
  "v_exp_f32 v228, v228\nv_mfma_f32_32x32x16_bf16 v[208:223], v[192:195], v[172:175], 0\nds_read_b128 v[184:187], %[lr0] offset:43008\nv_exp_f32 v229, v229\n" \
  "v_exp_f32 v230, v230\nv_exp_f32 v231, v231\nv_mfma_f32_32x32x16_bf16 v[208:223], v[196:199], v[168:171], v[208:223]\nv_exp_f32 v232, v232\n" \
  "v_exp_f32 v233, v233\nv_exp_f32 v234, v234\nv_exp_f32 v235, v235\nv_smfmac_f32_32x32x32_bf16 v[240:255], v[200:203], v[224:231], %[idx]\n" \
  "s_nop 1\nv_exp_f32 v236, v236\nv_exp_f32 v237, v237\nv_exp_f32 v238, v238\n" \
  "v_exp_f32 v239, v239\ns_setprio 0\nv_exp_f32 v208, v208\nv_exp_f32 v209, v209\n" \
  "s_waitcnt lgkmcnt(0)\nv_smfmac_f32_32x32x32_bf16 v[240:255], v[188:191], v[232:239], %[idx]\nds_read_b128 v[204:207], %[lr0] offset:48128\nv_exp_f32 v210, v210\n" \
  "v_exp_f32 v211, v211\nv_exp_f32 v212, v212\nv_exp_f32 v213, v213\nv_exp_f32 v214, v214\n" \
  "v_exp_f32 v215, v215\nv_exp_f32 v216, v216\nv_exp_f32 v217, v217\nv_exp_f32 v218, v218\n" \
  "v_exp_f32 v219, v219\nv_smfmac_f32_32x32x32_bf16 v[240:255], v[184:187], v[208:215], %[idx]\nv_exp_f32 v220, v220\nv_exp_f32 v221, v221\n" \
  "v_exp_f32 v222, v222\nv_exp_f32 v223, v223\ns_setprio 0\ns_waitcnt vmcnt(0) lgkmcnt(0)\n" \
  "s_mov_b32 m0, %[keep]\nv_smfmac_f32_32x32x32_bf16 v[240:255], v[204:207], v[216:223], %[idx]\ns_nop 15\n" \
  ""

#define LEAD 24
#define BD 3
constexpr int NREG = 43;
#define NW2 4

#define SUB4(V, i) __builtin_shufflevector(V, V, 4 * (i), 4 * (i) + 1, 4 * (i) + 2, 4 * (i) + 3)
#define ATT_CLOB "v176","v177","v178","v179","v180","v181","v182","v183","v184","v185","v186","v187","v188","v189","v190","v191", \
  "v192","v193","v194","v195","v196","v197","v198","v199","v200","v201","v202","v203","v204","v205","v206","v207", \
  "v208","v209","v210","v211","v212","v213","v214","v215","v216","v217","v218","v219","v220","v221","v222","v223", \
  "v224","v225","v226","v227","v228","v229","v230","v231","v232","v233","v234","v235","v236","v237","v238","v239","scc","memory"

DI u32x4 reduce_rows(rsrc_t rs, int Ly, int g, int j, int w, int l16, u32x4 x1own, u32x4 bb2) {
  u32x4 p[4], xb;
#pragma unroll
  for (int s = 0; s < 4; ++s) p[s] = ldg_sc1(rs, w * 1024 + l16, PF_OFF + (Ly * 64 + g) * PG + (s * 4 + j) * 7168);
#pragma unroll
  for (int e = 0; e < 4; ++e) {
    const float lo = (bf_lo(p[0][e]) + bf_lo(p[1][e])) + (bf_lo(p[2][e]) + bf_lo(p[3][e])) + (bf_lo(bb2[e]) + bf_lo(x1own[e]));
    const float hi = (bf_hi(p[0][e]) + bf_hi(p[1][e])) + (bf_hi(p[2][e]) + bf_hi(p[3][e])) + (bf_hi(bb2[e]) + bf_hi(x1own[e]));
    xb[e] = pk(lo, hi);
  }
  return xb;
}
DI u32x4 ext_frag(u32x4 c0, u32x4 c1, u32 bias, int h) {
  const float t = (__uint_as_float(c0[0]) + __uint_as_float(c0[1])) + (__uint_as_float(c0[2]) + __uint_as_float(c0[3])) + (__uint_as_float(c1[0]) + __uint_as_float(c1[1]));
  const float u = h ? t : t + (__uint_as_float(c1[2]) + __uint_as_float(c1[3]));
  const float cs = swap32_sum(u, u);
  const u32 ch = pk(cs, cs);
  return h ? u32x4{0u, 0u, 0u, 0u} : u32x4{ch, (pk(cs - bf_lo(ch), 0.f) & 0xffffu) | (bias << 16), bias, 0u};
}

template <bool G>
DI void run_layers(const In& in, u16* out, unsigned char* ws, LAS unsigned char* L, const int w_) {
  const rsrc_t rs = __builtin_amdgcn_make_buffer_rsrc(ws, 0, WS_TOTAL, 0x00020000);
  const u32x4 srd = make_srd(ws, WS_TOTAL);
  const u32 lds0 = (u32)(size_t)L;
  int lane, l16, h; LAS unsigned char *Lxn, *Lex, *Llo;
#define LANE_VALUES() do { lane = __builtin_amdgcn_mbcnt_hi(~0u, __builtin_amdgcn_mbcnt_lo(~0u, 0u)); PIN(lane); l16 = lane * 16; h = lane >> 5; \
    Lxn = L + L_XN + l16; Lex = L + L_EX + l16; Llo = L + l16; asm volatile("" : "+v"(Lxn), "+v"(Lex), "+v"(Llo)); } while (0)
  u32x4 hq[14], hkv[7];
  u64 tag = 0;
  if constexpr (!G) {
    __builtin_amdgcn_s_setprio(3);
    LANE_VALUES();
    const int b = blockIdx.x, id = ((b & 7) * 8 + (b >> 5)) * 4 + ((b >> 3) & 3);
    tag = __hip_atomic_load((GAS u64*)(ws + EPOCH_OFF), RLX_AGENT) + 1ull;
    if (lane < 8) stg_sc1(u32x4{0u, 0u, 0u, 0u}, rs, lane * 128, FLAG_OFF + id * 1024);
    *(LAS u32*)(L + L_RDY) = 0u;
#pragma unroll
    for (int i = 0; i < 7; ++i) hkv[i] = u32x4{0u, 0u, 0u, 0u};
  }
#pragma unroll 1
  for (int Ly = 0; Ly < 2; ++Ly) {
    int b = blockIdx.x, w = w_;
    asm volatile("" : "+s"(b), "+s"(w));
    const int g = (b & 7) * 8 + (b >> 5), j = (b >> 3) & 3;
    const int wf1 = W1F_OFF + (14 * j + 2 * w) * 28 * 1024, wf2 = W2F_OFF + (14 * j + 2 * w) * 28 * 1024;
    LANE_VALUES();
    const int sb = Ly * 13; (void)sb;
    const f32x16 zero = {};
    u32x4 wqf[14];
    if (Ly == 0) {
      STAMP(0);
      if constexpr (G) first_phase<true>(in, ws, rs, L, g, j, w, lane, wqf, tag); else first_phase<false>(in, ws, rs, L, g, j, w, lane, hq, tag);
    } else {
      if constexpr (G) {
        const int wqb = WQF_OFF + ((4 * j + (w & 3)) * 28 + 14 * (w >> 2)) * 1024;
        u32x4 wkv[7];
        YIELD();
        const u32x4 x1own = ldg_sc1(rs, ((8 * w + (lane >> 3)) * 32 + 8 * j + (lane & 7)) * 16, X1F_OFF + g * XG);
        const u32x4 bb2 = *(const u32x4*)(in.b2 + (8 * w + (lane >> 3)) * 8);
#pragma unroll
        for (int i = 0; i < 7; ++i) wkv[i] = ldf(rs, l16, WKVF_OFF + ((w & 3) * 14 + (w >> 2) * 7) * 1024, i);
        BAR();
        const u32x4 xb = reduce_rows(rs, 0, g, j, w, l16, x1own, bb2);
        STAMP(11);
        ln_kv_tail<true>(xb, wkv, rs, L, g, j, w, lane);
        FENCE();
#pragma unroll
        for (int i = 0; i < 5; ++i) wqf[i] = ldf(rs, l16, wqb, i);
        WAIT_BAR(5);
        STAMP(12);
        YIELD();
#pragma unroll
        for (int i = 5; i < 14; ++i) wqf[i] = ldf(rs, l16, wqb, i);
        BAR();
        STAMP(13);
        dma<4>(srd, l16, XNF_OFF + g * XG + w * 4096, lds0 + L_XN + w * 4096);
      } else {
        signal_and_wait(ws, E_P, g, j, lane, tag);
        BAR();
        STAMP(11);
        ln_kv_tail<false>(u32x4{0u, 0u, 0u, 0u}, hkv, rs, L, g, j, w, lane);
        BAR();
        STAMP(12);
        if (lane == 0) __hip_atomic_store((GAS u64*)(ws + KVF_OFF) + (64 + g) * 16 + j, tag, RLX_AGENT);
        signal_and_wait(ws, E_X, g, j, lane, tag);
        BAR();
        STAMP(13);
      }
      WAIT_BAR(0);
    }
    asm volatile("" : "+s"(w));
    STAMP(sb + 1);
    f32x16 qa;
    {
      u32x4 kf0, kf1;
      if constexpr (!G) { kf0 = ldg_sc1(rs, lane * 128, KVF_OFF + Ly * 8192); kf1 = ldg_sc1(rs, lane * 128 + 16, KVF_OFF + Ly * 8192); }
      u32x4 xq[14];
      LAS unsigned char* p[4];
#pragma unroll
      for (int m = 0; m < 4; ++m) { p[m] = L + L_XN + 14 * (w >> 2) * 1024 + h * 512 + (((lane & 31) ^ ((2 * (2 * (w >> 2) + m) + h) & 7)) * 16); asm volatile("" : "+v"(p[m])); }
#pragma unroll
      for (int i = 0; i < 14; ++i) xq[i] = lds128(p[i & 3], i * 1024);
      SBAR();
      qa = mfma32(G ? wqf[0] : hq[0], xq[0], zero);
#pragma unroll
      for (int i = 1; i < 14; ++i) qa = mfma32(G ? wqf[i] : hq[i], xq[i], qa);
#pragma unroll
      for (int i = 0; i < 4; ++i) *(LAS f32x4*)(Lex + w * 4096 + i * 1024) = f32x4{qa[4 * i], qa[4 * i + 1], qa[4 * i + 2], qa[4 * i + 3]};
      STAMP(43);
      if constexpr (!G) {
        const u32 tl = (u32)tag, th = (u32)(tag >> 32);
        for (u32 spins = 0;;) {
          if (__all(kf0[0] == tl && kf0[2] == tl && kf1[0] == tl && kf1[2] == tl && kf0[1] == th && kf0[3] == th && kf1[1] == th && kf1[3] == th) || spin_over(ws, spins, tag)) break;
          __builtin_amdgcn_s_sleep(4);
          FENCE();
          kf0 = ldg_sc1(rs, lane * 128, KVF_OFF + Ly * 8192); kf1 = ldg_sc1(rs, lane * 128 + 16, KVF_OFF + Ly * 8192);
        }
        __builtin_amdgcn_fence(__ATOMIC_ACQUIRE, "wavefront");
        if (b == 0 && Ly == 0 && lane == 0) __hip_atomic_store((GAS u64*)(ws + EPOCH_OFF), tag, RLX_AGENT);
      }
    }
    BAR();
    STAMP(44);
    const u32x4 skv = make_srd(ws + KV_OFF + Ly * 262144, 262144);
    const int t0 = ((b >> 3) * 2 + (w >> 2) * 32) & 63;
    const u32 mdst = lds0 + L_RING + w * 4096;
#define KV_START() do { \
      dma<2>(skv, l16, ((t0 + 62 + ((w >> 1) & 1)) & 63) * 4096 + (w & 1) * 2048, lds0 + L_RING + 65536 + (w >> 2) * 16384 + (w & 3) * 2048); \
      dma<4>(skv, l16, ((t0 + (w & 3)) & 63) * 4096, mdst); } while (0)
    KV_START();
    u32x4 q0, q1;
#pragma unroll
    for (int i = 0; i < 4; ++i) {
      const f32x4 pp = *(LAS f32x4*)(Lex + (w ^ 4) * 4096 + i * 1024);
#pragma unroll
      for (int e = 0; e < 4; ++e) qa[4 * i + e] = (qa[4 * i + e] + pp[e]) * QSCALE;
    }
#pragma unroll
    for (int i = 0; i < 4; ++i) { q0[i] = pk(qa[2 * i], qa[2 * i + 1]); q1[i] = pk(qa[8 + 2 * i], qa[9 + 2 * i]); }
    if (h) { q1[2] = 0u; q1[3] = 0u; }
    STAMP(sb + 2);
    u32x32 wA, wB, wC, wD; u32x16 wE;
    bool again = true;
    for (;;) {
      f32x16 o;
      {
        u32 ko = ((t0 + (w & 3)) & 63) * 4096, keep, tmp;
        LAS unsigned char* lr0 = Llo + L_RING + (w >> 2) * 16384; LAS unsigned char* lr2 = lr0 + 65536;
        if constexpr (G) {
          asm volatile(ATT_ASM_G
                       : "=&{v[240:255]}"(o), "=&{v[0:31]}"(wA), "=&{v[32:63]}"(wB), "=&{v[64:95]}"(wC), "=&{v[96:127]}"(wD), "=&{v[128:143]}"(wE),
                         [keep] "=&s"(keep), [t] "=&s"(tmp), [ko] "+s"(ko)
                       : "{v[172:175]}"(q0), "{v[168:171]}"(q1), [lr0] "v"(lr0), [lr2] "v"(lr2), [l16] "v"(l16), [idx] "v"(0xDDDDDDDDu),
                         [rkv] "s"(skv), [rw] "s"(srd), [m] "s"(mdst), [wo] "s"(WOF_OFF + (7 * j + w) * 16 * 1024), [w1] "s"(wf1)
                       : ATT_CLOB);
        } else {
          asm volatile(ATT_ASM_H
                       : "=&{v[240:255]}"(o), [keep] "=&s"(keep), [ko] "+s"(ko)
                       : "{v[172:175]}"(q0), "{v[168:171]}"(q1), [lr0] "v"(lr0), [lr2] "v"(lr2), [l16] "v"(l16), [idx] "v"(0xDDDDDDDDu),
                         [rkv] "s"(skv), [m] "s"(mdst)
                       : ATT_CLOB);
          __builtin_amdgcn_s_setprio(3);
        }
      }
      LANE_VALUES();
      STAMP(sb + 3);
      f32x4 s0, s1, a0, a1;
      if (w < 4) { s0 = f32x4{o[8], o[9], o[10], o[11]}; s1 = f32x4{o[12], o[13], o[14], o[15]}; a0 = f32x4{o[0], o[1], o[2], o[3]}; a1 = f32x4{o[4], o[5], o[6], o[7]}; }
      else { s0 = f32x4{o[0], o[1], o[2], o[3]}; s1 = f32x4{o[4], o[5], o[6], o[7]}; a0 = f32x4{o[8], o[9], o[10], o[11]}; a1 = f32x4{o[12], o[13], o[14], o[15]}; }
      *(LAS f32x4*)(Lex + w * 4096) = s0;
      *(LAS f32x4*)(Lex + w * 4096 + 1024) = s1;
      *(LAS float*)(L + L_EX + w * 4096 + 2048 + lane * 4) = o[12];
      const u32 unsafe = (u32)(__ballot(!(o[12] >= 0x1p-60f && o[12] <= 0x1p59f)) >> 32);
      *(LAS u32*)(L + L_BAD + w * 4) = unsafe;
      BAR();
      const u32x4 f0 = lds128(L, L_BAD), f1 = lds128(L, L_BAD + 16);
      const f32x4 p0 = *(LAS f32x4*)(Lex + (w ^ 4) * 4096), p1 = *(LAS f32x4*)(Lex + (w ^ 4) * 4096 + 1024);
      const int tok = lane & 31;
      const float lsum = *(LAS float*)(L + L_EX + (w & 3) * 4096 + 2176 + tok * 4) + *(LAS float*)(L + L_EX + ((w & 3) + 4) * 4096 + 2176 + tok * 4);
      const float rl = __builtin_amdgcn_rcpf(lsum);
      a0 = (a0 + p0) * rl; a1 = (a1 + p1) * rl;
      const u32x4 ob = {pk(a0[0], a0[1]), pk(a0[2], a0[3]), pk(a1[0], a1[1]), pk(a1[2], a1[3])};
      stg_sc1(ob, rs, (4 * j + (w & 3)) * 2048 + (tok >> 4) * 1024 + ((2 * (w >> 2) + h) * 16 + (tok & 15)) * 16, OF_OFF + (Ly * 64 + g) * 32768);
      const u32 bad = f0[0] | f0[1] | f0[2] | f0[3] | f1[0] | f1[1] | f1[2] | f1[3];
      if (__builtin_expect(!(again && __builtin_amdgcn_readfirstlane(bad) != 0u), 1)) break;
      again = false;
      float mx = -3.0e38f;
#pragma unroll 1
      for (int t = 0; t < 64; ++t) {
        const u32x4 k0 = ldg_sc1(rs, l16, KV_OFF + Ly * 262144 + t * 4096), k1 = ldg_sc1(rs, l16, KV_OFF + Ly * 262144 + t * 4096 + 1024);
        f32x16 s = mfma32(k0, q0, zero);
        s = mfma32(k1, q1, s);
#pragma unroll
        for (int i = 0; i < 16; ++i) mx = fmaxf(mx, s[i]);
      }
      const float m = xor32_max(mx);
      const u32 mh = pk(-m, 0.f) & 0xffffu; const float r1 = -m - bf_lo(mh);
      const u32 mm = pk(r1, 0.f) & 0xffffu; const float r2 = r1 - bf_lo(mm);
      const u32 ml = pk(r2, 0.f) & 0xffffu;
      if (h) { q1[2] = mh | (mm << 16); q1[3] = ml; }
      KV_START();
    }
    u32x4 wf[112];
#define FS2(k) ((k) < 28 ? (((k) >> 2) * 4 + (((k) >> 1) & 1)) : ((((k) % 14) >> 1) * 4 + 2 + ((k) & 1)))
#define NT2(k) ((k) < 28 ? ((k) & 1) : ((k) >= 42))
#define WLOAD(i) wf[i] = ((i) < 56) ? ldf(rs, l16, wf1, (i)) : ldf(rs, l16, wf2, NT2((i) - 56) * 28 + FS2((i) - 56))
#define WLOADS(a, z) _Pragma("unroll") for (int i_ = (a); i_ < (z); ++i_) WLOAD(i_)
    const u32 st0 = lds0 + L_STASH + w * 9216, st1 = lds0 + L_EX + w * 4096;
    FENCE();
    if constexpr (G) {
      wf[0] = SUB4(wC, 0); wf[1] = SUB4(wC, 1); wf[2] = SUB4(wC, 2); wf[3] = SUB4(wC, 3); wf[4] = SUB4(wC, 4); wf[5] = SUB4(wC, 5); wf[6] = SUB4(wC, 6); wf[7] = SUB4(wC, 7);
      wf[8] = SUB4(wD, 0); wf[9] = SUB4(wD, 1); wf[10] = SUB4(wD, 2); wf[11] = SUB4(wD, 3); wf[12] = SUB4(wD, 4); wf[13] = SUB4(wD, 5); wf[14] = SUB4(wD, 6); wf[15] = SUB4(wD, 7);
      wf[16] = SUB4(wE, 0); wf[17] = SUB4(wE, 1); wf[18] = SUB4(wE, 2); wf[19] = SUB4(wE, 3);
      WLOADS(20, 24);
      WAIT_BAR(4);
      STAMP(sb + 4);
      YIELD();
      const rsrc_t rx = __builtin_amdgcn_make_buffer_rsrc(Ly ? (void*)(ws + XF_OFF) : (void*)in.x, 0, 2048 * D_ * 2, 0x00020000);
      u32x2 xres[2];
#pragma unroll
      for (int t = 0; t < 2; ++t) xres[t] = ldg64_sc1(rx, (16 * t + (lane & 15)) * (D_ * 2) + (lane >> 4) * 8, g * 32 * D_ * 2 + (7 * j + w) * 32);
      dma<4>(srd, l16, wf1 + 43 * 1024, st0);
      dma<4>(srd, l16, wf1 + 47 * 1024, st0 + 4096);
      STAMP(36);
      BAR();
      STAMP(37);
      dma<4>(srd, l16, OF_OFF + (Ly * 64 + g) * 32768 + w * 4096, lds0 + L_OF + w * 4096);
      WAIT_BAR(0);
      STAMP(sb + 5);
      const u32x4 wof[16] = {SUB4(wA, 0), SUB4(wA, 1), SUB4(wA, 2), SUB4(wA, 3), SUB4(wA, 4), SUB4(wA, 5), SUB4(wA, 6), SUB4(wA, 7),
                             SUB4(wB, 0), SUB4(wB, 1), SUB4(wB, 2), SUB4(wB, 3), SUB4(wB, 4), SUB4(wB, 5), SUB4(wB, 6), SUB4(wB, 7)};
      f32x4 a0 = {bf_lo(xres[0][0]), bf_hi(xres[0][0]), bf_lo(xres[0][1]), bf_hi(xres[0][1])}, a1 = {bf_lo(xres[1][0]), bf_hi(xres[1][0]), bf_lo(xres[1][1]), bf_hi(xres[1][1])};
      u32x4 bf[6];
#pragma unroll
      for (int i = 0; i < 6; ++i) bf[i] = lds128(Llo, L_OF + i * 1024);
      SBAR();
#pragma unroll
      for (int i = 0; i < 32; ++i) {
        const u32x4 bb = bf[i % 6];
        if (i & 1) a1 = mfma16(wof[i >> 1], bb, a1); else a0 = mfma16(wof[i >> 1], bb, a0);
        if (i + 6 < 32) bf[i % 6] = lds128(Llo, L_OF + (i + 6) * 1024);
        if (i % 3 == 0 && i < 18) WLOAD(24 + i / 3);
        SBAR();
      }
      STAMP(38);
      {
        const auto r0 = __builtin_amdgcn_permlane16_swap(pk(a0[0], a0[1]), pk(a1[0], a1[1]), false, false);
        const auto r1 = __builtin_amdgcn_permlane16_swap(pk(a0[2], a0[3]), pk(a1[2], a1[3]), false, false);
        stg_sc1(u32x4{r0[0], r1[0], r0[1], r1[1]}, rs, l16, X1F_OFF + (Ly * 64 + g) * XG + (7 * j + w) * 1024);
        const float u = swap32_sum((a0[0] + a0[1]) + (a0[2] + a0[3]), (a1[0] + a1[1]) + (a1[2] + a1[3]));
        const float v = swap32_sum((a0[0] * a0[0] + a0[1] * a0[1]) + (a0[2] * a0[2] + a0[3] * a0[3]), (a1[0] * a1[0] + a1[1] * a1[1]) + (a1[2] * a1[2] + a1[3] * a1[3]));
        stg32_sc1(__float_as_uint(swap16_sum(u, v)), rs, lane * 4, SX_OFF + (Ly * 64 + g) * 7168 + (7 * j + w) * 256);
      }
      FENCE();
      STAMP(39);
      WLOADS(30, 34);
      WAIT_BAR(4);
      STAMP(sb + 6);
      YIELD();
      const u32x4 ca0 = ldg_sc1(rs, (lane & 31) * 64 + h * 32, CS_OFF + (14 * j + 2 * w) * 2048), ca1 = ldg_sc1(rs, (lane & 31) * 64 + h * 32 + 16, CS_OFF + (14 * j + 2 * w) * 2048);
      const u32x4 cb0 = ldg_sc1(rs, (lane & 31) * 64 + h * 32 + 2048, CS_OFF + (14 * j + 2 * w) * 2048), cb1 = ldg_sc1(rs, (lane & 31) * 64 + h * 32 + 2064, CS_OFF + (14 * j + 2 * w) * 2048);
      const u32 bia = in.b1[(14 * j + 2 * w) * 32 + (lane & 31)], bib = in.b1[(14 * j + 2 * w) * 32 + 32 + (lane & 31)];
      WLOADS(34, 40);
      STAMP(41);
      BAR();
      STAMP(42);
      dma<4>(srd, l16, X1F_OFF + (Ly * 64 + g) * XG + w * 4096, lds0 + L_XN + w * 4096);
      const u32x4 extA = ext_frag(ca0, ca1, bia, h), extB = ext_frag(cb0, cb1, bib, h);
      WAIT_BAR(0);
      STAMP(sb + 7); STAMP(sb + 8);
      LAS unsigned char* Ls0 = Llo + L_STASH + w * 9216; LAS unsigned char* Ls1 = Lex + w * 4096;
      u32x4 bext; u32 hp[8]; float rstd;
      f32x16 hA, hB, yA, yB;
      constexpr int K0 = 6;
#define WSTEP(s) do { if ((s) >= K0) { const int k_ = (((s) - K0) * 55 + (110 - LEAD - K0)) / (111 - LEAD - K0); if (k_ < 56 && K0 + k_ * (111 - LEAD - K0) / 55 == (s)) WLOAD(56 + k_); } } while (0)
#pragma unroll
      for (int i = 0; i < BD; ++i) bf[i] = lds128(Lxn, i * 1024);
      SBAR();
#pragma unroll
      for (int i = 0; i < 56; ++i) {
        const u32x4 bb = bf[i % BD];
        if (i == 0) hA = mfma32(wf[i], bb, zero); else if (i < 28) hA = mfma32(wf[i], bb, hA); else if (i == 28) hB = mfma32(wf[i], bb, zero); else hB = mfma32(wf[i], bb, hB);
        if (i + BD < 56) bf[i % BD] = lds128(Lxn, ((i + BD) % 28) * 1024);
        if (i < 3) WLOAD(40 + i);
        if (i == 3) dma<1>(srd, l16, wf1 + 51 * 1024, st0 + 8192);
        if (i == 4) dma<2>(srd, l16, wf1 + 52 * 1024, st1);
        if (i == 5) dma<2>(srd, l16, wf1 + 54 * 1024, st1 + 2048);
        if (i + BD == NREG) asm volatile("s_waitcnt vmcnt(%0)" :: "n"(12) : "memory");
        if (i + BD >= NREG && i + BD < 56) wf[i + BD] = (i + BD < 52) ? lds128(Ls0, (i + BD - NREG) * 1024) : lds128(Ls1, (i + BD - 52) * 1024);
        WSTEP(i);
        if (i == 27) {
          u32 t_, rdy;
          SBAR();
          asm volatile("1:\n\tds_read_b32 %0, %4\n\tds_read_b128 %1, %5\n\tds_read_b32 %2, %6\n\ts_waitcnt lgkmcnt(0)\n\t"
                       "v_readfirstlane_b32 %3, %0\n\ts_cmp_lg_u32 %3, %7\n\ts_cbranch_scc1 1b" : "=&v"(rdy), "=&v"(bext), "=&v"(rstd), "=&s"(t_)
                       : "v"(L + L_RDY), "v"(Llo + L_EXT), "v"(L + L_RSTD + (lane & 31) * 4), "s"(Ly + 1) : "scc", "memory");
          SBAR();
          hA = mfma32(extA, bext, hA);
        }
        if (i == 29) hB = mfma32(extB, bext, hB);
        if (i >= 30 && i < 54 && (i - 30) % 3 == 0) { const int e = (i - 30) / 3; hp[e] = gelu_pk(hA[2 * e], hA[2 * e + 1], rstd); PIN(hp[e]); }
        if (i == 41 || i == 53) sts128(Llo + w * 4096, L_H + (i == 53) * 1024, i == 53 ? u32x4{hp[4], hp[5], hp[6], hp[7]} : u32x4{hp[0], hp[1], hp[2], hp[3]});
        SBAR();
      }
      BAR();
      STAMP(sb + 9);
      const int po = PF_OFF + (Ly * 64 + g) * PG + j * 4 * 7168 + ((lane & 31) >> 3) * 7168 + ((8 * w + h) * 8 + (lane & 7)) * 16;
#define PSTORE(y, t, s) stg_sc1(u32x4{pk(y[8 * s], y[8 * s + 1]), pk(y[8 * s + 2], y[8 * s + 3]), pk(y[8 * s + 4], y[8 * s + 5]), pk(y[8 * s + 6], y[8 * s + 7])}, rs, po + (4 * t + 2 * s) * 128, 0)
      bf[0] = lds128(Llo, L_H + FS2(0) * 1024); bf[1] = lds128(Llo, L_H + FS2(2) * 1024);
      SBAR();
#pragma unroll
      for (int i = 0; i < 28; ++i) {
        const u32x4 a = wf[56 + i], bb = bf[(i >> 1) & 1];
        if (i == 0) yA = mfma32(a, bb, zero); else if (i == 1) yB = mfma32(a, bb, zero); else if (i & 1) yB = mfma32(a, bb, yB); else yA = mfma32(a, bb, yA);
        if ((i & 1) && i + 3 < 28) bf[(i >> 1) & 1] = lds128(Llo, L_H + FS2(i + 3) * 1024);
        WSTEP(56 + i);
        if (i < 24 && i % 3 == 0) { const int e = i / 3; hp[e] = gelu_pk(hB[2 * e], hB[2 * e + 1], rstd); PIN(hp[e]); }
        if (i == 11 || i == 23) sts128(Llo + w * 4096, L_H + (2 + (i == 23)) * 1024, i == 23 ? u32x4{hp[4], hp[5], hp[6], hp[7]} : u32x4{hp[0], hp[1], hp[2], hp[3]});
        SBAR();
      }
      BAR();
#pragma unroll
      for (int i = 0; i < BD; ++i) bf[i] = lds128(Llo, L_H + FS2(28 + i) * 1024);
      SBAR();
#pragma unroll
      for (int i = 28; i < 56; ++i) {
        const u32x4 a = wf[56 + i], bb = bf[(i - 28) % BD];
        if (i < 42) yA = mfma32(a, bb, yA); else yB = mfma32(a, bb, yB);
        if (i + BD < 56) bf[(i - 28) % BD] = lds128(Llo, L_H + FS2(i + BD) * 1024);
        WSTEP(56 + i);
        if (i == 44) PSTORE(yA, 0, 0);
        if (i == 45) PSTORE(yA, 0, 1);
        SBAR();
      }
      STAMP(45);
      PSTORE(yB, 1, 0); PSTORE(yB, 1, 1);
    } else {
      WAIT_BAR(0);
      STAMP(sb + 4);
      signal_and_wait(ws, 4 * Ly + E_O, g, j, lane, tag);
      STAMP(36);
      BAR();
      STAMP(37);
      dma<4>(srd, l16, OF_OFF + (Ly * 64 + g) * 32768 + w * 4096, lds0 + L_OF + w * 4096);
      WAIT_BAR(0);
      STAMP(sb + 5);
      BAR();
      STAMP(sb + 6);
      signal_and_wait(ws, 4 * Ly + E_X1, g, j, lane, tag);
      STAMP(41);
      BAR();
      STAMP(42);
      u32x4 sx[7];
#pragma unroll
      for (int i = 0; i < 7; ++i) sx[i] = ldf(rs, l16, SX_OFF + (Ly * 64 + g) * 7168, i);
      BAR();
      STAMP(sb + 7); STAMP(sb + 8);
      f32x4 t = __builtin_bit_cast(f32x4, sx[0]);
#pragma unroll
      for (int i = 1; i < 7; ++i) t += __builtin_bit_cast(f32x4, sx[i]);
#pragma unroll
      for (int e = 0; e < 4; ++e) { t[e] = swap32_sum(t[e], t[e]); t[e] = swap16_sum(t[e], t[e]); }
      *(LAS f32x4*)(L + L_SUM + (lane & 15) * 16) = t;
      asm volatile("s_waitcnt lgkmcnt(0)" ::: "memory");
      const int so = L_SUM + ((lane & 16) * 2 + (lane & 15)) * 4;
      const float mean = *(LAS float*)(L + so) * (1.f / D_);
      const float ve = fmaxf(*(LAS float*)(L + so + 64) * (1.f / D_) - mean * mean, 0.f) + 1e-5f;
      const float rstd = __builtin_amdgcn_rsqf(ve), sd = ve * rstd;
      const u32 mh = pk(-mean, sd);
      const u32 lo = pk(-mean - bf_lo(mh), sd - bf_hi(mh));
      sts128(Llo, L_EXT, h ? u32x4{0u, 0u, 0u, 0u} : u32x4{(mh & 0xffffu) | (lo << 16), mh, lo >> 16, 0u});
      *(LAS float*)(L + L_RSTD + (lane & 31) * 4) = rstd;
      asm volatile("s_waitcnt lgkmcnt(0)" ::: "memory");
      *(volatile LAS u32*)(L + L_RDY) = Ly + 1;
#pragma unroll
      for (int i = 0; i < 7; ++i) hkv[i] = ldf(rs, l16, WKVF_OFF + (3 * 14 + 7) * 1024, i);
      BAR();
      STAMP(sb + 9);
      BAR();
    }
    WAIT_BAR(0);
    STAMP(sb + 10);
  }
  {
    const int b = blockIdx.x, w = w_, g = (b & 7) * 8 + (b >> 5), j = (b >> 3) & 3;
    LANE_VALUES();
    if constexpr (G) {
      YIELD();
      const u32x4 x1own = ldg_sc1(rs, ((8 * w + (lane >> 3)) * 32 + 8 * j + (lane & 7)) * 16, X1F_OFF + (64 + g) * XG);
      const u32x4 bb2 = *(const u32x4*)(in.b2 + (8 * w + (lane >> 3)) * 8);
      BAR();
      STAMP(24);
      const u32x4 xb = reduce_rows(rs, 1, g, j, w, l16, x1own, bb2);
      *(u32x4*)(out + (size_t)(g * 32 + j * 8 + (lane & 7)) * D_ + (8 * w + (lane >> 3)) * 8) = xb;
    } else {
      signal_and_wait(ws, 4 + E_P, g, j, lane, tag);
      BAR();
      STAMP(24);
    }
    STAMP(25); STAMP(26);
    STAMP_DUMP();
  }
}

__global__ __launch_bounds__(512, 2) void k_main(const u16* x, const u16* wq, const u16* wk, const u16* wv, const u16* wo, const u16* w1, const u16* b1,
                                                 const u16* w2, const u16* b2, u16* out, unsigned char* ws) {
  __shared__ __attribute__((aligned(16))) unsigned char smem[L_BYTES];
  LAS unsigned char* L = (LAS unsigned char*)smem;
  const In in = {x, wq, wk, wv, wo, w1, b1, w2, b2};
  const int w = __builtin_amdgcn_readfirstlane(threadIdx.x >> 6);
  if (w < 7) run_layers<true>(in, out, ws, L, w); else run_layers<false>(in, out, ws, L, w);
}

extern "C" void kernel_launch(void* const* d_in, const int* in_sizes, int n_in, void* d_out, int out_size, void* d_ws, size_t ws_size,
                              hipStream_t stream) {
  k_main<<<256, 512, 0, stream>>>((const u16*)d_in[0], (const u16*)d_in[1], (const u16*)d_in[2], (const u16*)d_in[3], (const u16*)d_in[4],
                                  (const u16*)d_in[6], (const u16*)d_in[7], (const u16*)d_in[8], (const u16*)d_in[9], (u16*)d_out, (unsigned char*)d_ws);
}
```
